```python
import torch

def _softmax(x, axis=-1):
    return torch.softmax(x, dim=axis)
def _gelu(x):
    return torch.nn.functional.gelu(x, approximate="tanh")
def _silu(x):
    return torch.nn.functional.silu(x)
def _sigmoid(x):
    return torch.sigmoid(x)
def _layernorm(x, axis=-1, eps=1e-5):
    m = x.mean(dim=axis, keepdim=True)
    v = x.var(dim=axis, keepdim=True, unbiased=False)
    return (x - m) / torch.sqrt(v + eps)
def _scatter_add(tgt, idx, src, axis=0):
    return tgt.clone().index_add_(axis, idx, src)
def _segment_sum(src, seg, n):
    out = torch.zeros((n,) + src.shape[1:], dtype=src.dtype)
    return out.index_add_(0, seg, src)
def _pad(x, pw):
    flat = tuple(v for pair in reversed(pw) for v in pair)
    return torch.nn.functional.pad(x, flat)

kernel_name = 'concat_heads__mha_block__softmax_full_mha__prompt'

def setup_inputs():

    _w = (lambda a: (lambda t: t.to(torch.bfloat16) if t.is_floating_point() else t)(torch.as_tensor(a)))
    def _A_setup():
        B, S, D, H = 1, 2048, 448, 16
        return {
            "x": _w((lambda *s: torch.randn(*s, dtype=torch.bfloat16) * 0.2)(B, S, D)),
            "wqkv": _w((lambda *s: torch.randn(*s, dtype=torch.bfloat16) * 0.2)(D, 3 * D)),
            "wo": _w((lambda *s: torch.randn(*s, dtype=torch.bfloat16) * 0.05)(D, D)),
        }
    _A_in = _A_setup()

    def _B_setup():
        B, S, D = 1, 2048, 448
        return {
            "x": _w((lambda *s: torch.randn(*s, dtype=torch.bfloat16) * 0.2)(B, S, D)),
            "wqkv": _w((lambda *s: torch.randn(*s, dtype=torch.bfloat16) * 0.2)(D, 3 * D)),
            "wo": _w((lambda *s: torch.randn(*s, dtype=torch.bfloat16) * 0.05)(D, D)),
        }
    _B_in = _B_setup()

    return {
        "A_x": _A_in["x"],
        "A_wqkv": _A_in["wqkv"],
        "A_wo": _A_in["wo"],
        "B_x": _B_in["x"],
        "B_wqkv": _B_in["wqkv"],
        "B_wo": _B_in["wo"],
    }

def reference(A_x, A_wqkv, A_wo, B_x, B_wqkv, B_wo):
    def _A_ref(x, wqkv, wo):
        B, S, D = x.shape
        H = 16
        Dh = D // H
        qkv = (lambda x, *d: x.permute(*d))((x @ wqkv).reshape(B, S, 3, H, Dh), 2, 0, 3, 1, 4)
        q, k, v = qkv[0], qkv[1], qkv[2]
        scores = torch.einsum("bhqd,bhkd->bhqk", q, k) * (Dh ** -0.5)
        p = _softmax(scores, axis=-1)
        o = torch.einsum("bhqk,bhkd->bhqd", p, v)
        return (lambda x, *d: x.permute(*d))(o, 0, 2, 1, 3).reshape(B, S, D) @ wo

    def _B_ref(x, wqkv, wo):
        B, S, D = x.shape
        H = 16; Dh = D // H
        qkv = (lambda x, *d: x.permute(*d))((x @ wqkv).reshape(B, S, 3, H, Dh), 2, 0, 3, 1, 4)
        q, k, v = qkv[0], qkv[1], qkv[2]
        scale = Dh ** -0.5
        scores = torch.einsum("bhqd,bhkd->bhqk", q, k) * scale
        p = _softmax(scores, axis=-1)
        o = torch.einsum("bhqk,bhkd->bhqd", p, v)
        return (lambda x, *d: x.permute(*d))(o, 0, 2, 1, 3).reshape(B, S, D) @ wo

    a = _A_ref(A_x, A_wqkv, A_wo)
    b = _B_ref(B_x, B_wqkv, B_wo)
    return torch.concatenate([a, b], axis=-1)
```

```cpp
#include <hip/hip_runtime.h>
#include <hip/hip_bf16.h>
#include <stdint.h>

typedef __attribute__((ext_vector_type(8))) short bf16x8;
typedef __attribute__((ext_vector_type(4))) short s16x4;
typedef __attribute__((ext_vector_type(16))) float f32x16;
typedef __attribute__((ext_vector_type(4))) unsigned u32x4;
typedef __attribute__((ext_vector_type(2))) unsigned u32x2;
typedef __attribute__((ext_vector_type(2))) float f32x2;
typedef __attribute__((ext_vector_type(2))) __bf16 b16x2;
typedef __attribute__((address_space(3))) char* lds_ptr;
typedef __attribute__((address_space(3))) const char* lds_cptr;

#define DI __device__ __forceinline__
#define MFMA(a, b, c) __builtin_amdgcn_mfma_f32_32x32x16_bf16((a), (b), (c), 0, 0, 0)

constexpr int SEQ = 2048, DM = 448, NH = 16, DH = 28, N3 = 1344, NTILE = 64;
constexpr size_t QF_OFF = 0;
constexpr size_t KV_OFF = (size_t)4 << 20;
constexpr size_t OF_OFF = (size_t)12 << 20;
constexpr float QSCALE = 0.18898223650461362f * 1.4426950408889634f;

DI unsigned pk2(float a, float b) { f32x2 t = {a, b}; b16x2 r = __builtin_convertvector(t, b16x2); return __builtin_bit_cast(unsigned, r); }
DI bf16x8 pack8(const f32x16& x, int s) {
    u32x4 p = {pk2(x[8 * s], x[8 * s + 1]), pk2(x[8 * s + 2], x[8 * s + 3]), pk2(x[8 * s + 4], x[8 * s + 5]), pk2(x[8 * s + 6], x[8 * s + 7])};
    return __builtin_bit_cast(bf16x8, p);
}
DI void glds16(const void* g, lds_ptr l) { __builtin_amdgcn_global_load_lds((const __attribute__((address_space(1))) unsigned*)g, (__attribute__((address_space(3))) unsigned*)l, 16, 0, 0); }
DI bf16x8 ldsr16(lds_cptr p) { return *(const __attribute__((address_space(3))) bf16x8*)p; }
DI s16x4 ldstr(lds_cptr p) { return __builtin_bit_cast(s16x4, __builtin_amdgcn_ds_read_tr16_b64_v4i16((__attribute__((address_space(3))) s16x4*)p)); }
DI bf16x8 cat4(s16x4 a, s16x4 b) { return __builtin_shufflevector(a, b, 0, 1, 2, 3, 4, 5, 6, 7); }
#define WAITV(n) asm volatile("s_waitcnt vmcnt(" #n ")" ::: "memory")
#define BAR() do { asm volatile("s_waitcnt lgkmcnt(0)" ::: "memory"); __builtin_amdgcn_s_barrier(); asm volatile("" ::: "memory"); } while (0)
DI float swap_halves(float v) {
    auto r = __builtin_amdgcn_permlane32_swap(__float_as_uint(v), __float_as_uint(v), false, false);
    return (threadIdx.x & 32) ? __uint_as_float(r[0]) : __uint_as_float(r[1]);
}

constexpr int G1_XB = 16384, G1_WB = 24576, G1_STG = G1_XB + G1_WB, G1_LDS = 3 * G1_STG;
__global__ __launch_bounds__(512) void qkv_kernel(const __hip_bfloat16* __restrict__ xA, const __hip_bfloat16* __restrict__ wA,
                                                  const __hip_bfloat16* __restrict__ xB, const __hip_bfloat16* __restrict__ wB, char* __restrict__ ws) {
    __shared__ __attribute__((aligned(16))) char smem[G1_LDS];
    const lds_ptr lds = (lds_ptr)smem;
    const int tid = threadIdx.x, lane = tid & 63, w = __builtin_amdgcn_readfirstlane(tid >> 6);
    const int bid = blockIdx.x, xcd = bid & 7, idx = bid >> 3;
    const int p = xcd >> 2, mt = 4 * (xcd & 3) + (idx >> 3), hj = idx & 7;
    const __hip_bfloat16* x = p ? xB : xA; const __hip_bfloat16* W = p ? wB : wA;
    const __hip_bfloat16* xsrc[2]; const __hip_bfloat16* wsrc[3];
#pragma unroll
    for (int i = 0; i < 2; ++i) { const int row = 8 * (2 * w + i) + (lane >> 3), c = (lane & 7) ^ ((row >> 1) & 7); xsrc[i] = x + (size_t)(mt * 128 + row) * DM + 8 * c; }
#pragma unroll
    for (int i = 0; i < 3; ++i) { int e = 64 * (3 * w + i) + lane; if (e >= 1344) e -= 1344; const int row = e / 21, cc = e % 21;
        wsrc[i] = W + (size_t)row * N3 + (cc / 7) * DM + hj * 56 + (cc % 7) * 8; }
#define G1_STAGE(kc, slot) do { _Pragma("unroll") for (int i = 0; i < 2; ++i) glds16(xsrc[i] + (kc) * 64, lds + (slot) * G1_STG + (2 * w + i) * 1024); \
        _Pragma("unroll") for (int i = 0; i < 3; ++i) glds16(wsrc[i] + (size_t)(kc) * 64 * N3, lds + (slot) * G1_STG + G1_XB + (3 * w + i) * 1024); } while (0)
    G1_STAGE(0, 0); G1_STAGE(1, 1);
    const int tl = w & 3, hs = w >> 2, h = lane >> 5, r32 = lane & 31;
    const int xrow = 32 * tl + r32, xsw = (xrow >> 1) & 7;
    const int wbase = (8 * h + ((lane & 15) >> 2)) * 336 + hs * 56 + ((lane >> 4) & 1) * 32 + (lane & 3) * 8;
    f32x16 aq = {}, ak = {}, av = {};
#pragma unroll
    for (int kc = 0; kc < 7; ++kc) {
        if (kc < 6) WAITV(5); else WAITV(0);
        BAR();
        if (kc + 2 < 7) G1_STAGE(kc + 2, (kc + 2) % 3);
        const lds_cptr xs = lds + (kc % 3) * G1_STG, wsb = xs + G1_XB + wbase;
#pragma unroll
        for (int ks = 0; ks < 4; ++ks) {
            const bf16x8 xf = ldsr16(xs + xrow * 128 + (((2 * ks + h) ^ xsw) * 16));
            const bf16x8 wq = cat4(ldstr(wsb + ks * 16 * 336), ldstr(wsb + (ks * 16 + 4) * 336));
            const bf16x8 wk = cat4(ldstr(wsb + ks * 16 * 336 + 112), ldstr(wsb + (ks * 16 + 4) * 336 + 112));
            const bf16x8 wv = cat4(ldstr(wsb + ks * 16 * 336 + 224), ldstr(wsb + (ks * 16 + 4) * 336 + 224));
            aq = MFMA(wq, xf, aq); ak = MFMA(wk, xf, ak); av = MFMA(xf, wv, av);
        }
    }
#undef G1_STAGE
    const int slot = p * 16 + 2 * hj + hs, T = mt * 4 + tl;
#pragma unroll
    for (int r = 0; r < 16; ++r) aq[r] *= QSCALE;
    if (h) {
#pragma unroll
        for (int r = 12; r < 16; ++r) { aq[r] = 0.f; ak[r] = 0.f; }
    }
    if (r32 >= 28) { const float f = (r32 == 28) ? 1.f : 0.f;
#pragma unroll
        for (int r = 0; r < 16; ++r) av[r] = f; }
    char* qd = ws + QF_OFF + ((size_t)(slot * 64 + T)) * 2048 + lane * 16;
    char* kd = ws + KV_OFF + ((size_t)(slot * 64 + T)) * 4096 + lane * 16;
    *(bf16x8*)(qd) = pack8(aq, 0); *(bf16x8*)(qd + 1024) = pack8(aq, 1);
    *(bf16x8*)(kd) = pack8(ak, 0); *(bf16x8*)(kd + 1024) = pack8(ak, 1);
    *(bf16x8*)(kd + 2048) = pack8(av, 0); *(bf16x8*)(kd + 3072) = pack8(av, 1);
}

constexpr int A_CH = 16384, A_GRP = 3 * A_CH, A_LDS = 2 * A_GRP + 256;
template <bool SAFE>
DI void attn_loop(const char* __restrict__ kvsrc, lds_ptr lds, int ldsg, int qb, int lane, bf16x8 q0, bf16x8 q1, f32x16& o, float& mref) {
    const char* src = kvsrc + qb * 2048 + lane * 16;
    const lds_ptr dst = lds + ldsg + qb * 2048;
#define A_STAGE(c) do { glds16(src + (size_t)(c) * A_CH, dst + ((c) % 3) * A_CH); glds16(src + (size_t)(c) * A_CH + 1024, dst + ((c) % 3) * A_CH + 1024); } while (0)
    A_STAGE(0); A_STAGE(1);
    const lds_cptr rd = lds + ldsg + lane * 16;
    f32x16 negm = {};
    float m = -INFINITY;
#pragma unroll
    for (int c = 0; c < 8; ++c) {
        if (c < 7) WAITV(2); else WAITV(0);
        BAR();
        if (c + 2 < 8) A_STAGE(c + 2);
        const lds_cptr cb = rd + (c % 3) * A_CH;
        if (!SAFE && c == 0) {
            const f32x16 z = {};
            f32x16 s = MFMA(ldsr16(cb), q0, z); s = MFMA(ldsr16(cb + 1024), q1, s);
            float t = s[0];
#pragma unroll
            for (int r = 1; r < 16; ++r) t = fmaxf(t, s[r]);
            t = fmaxf(t, swap_halves(t));
            m = t;
#pragma unroll
            for (int r = 0; r < 16; ++r) negm[r] = -t;
        }
#pragma unroll
        for (int t = 0; t < 4; ++t) {
            const bf16x8 k0 = ldsr16(cb + t * 4096), k1 = ldsr16(cb + t * 4096 + 1024), v0 = ldsr16(cb + t * 4096 + 2048), v1 = ldsr16(cb + t * 4096 + 3072);
            f32x16 s = MFMA(k0, q0, negm); s = MFMA(k1, q1, s);
            if (SAFE) {
                float tm = s[0];
#pragma unroll
                for (int r = 1; r < 16; ++r) tm = fmaxf(tm, s[r]);
                tm = fmaxf(tm, swap_halves(tm));
                const float mn = fmaxf(m, tm), al = __builtin_amdgcn_exp2f(m - mn);
                m = mn;
#pragma unroll
                for (int r = 0; r < 16; ++r) { o[r] *= al; s[r] -= mn; }
            }
#pragma unroll
            for (int r = 0; r < 16; ++r) s[r] = __builtin_amdgcn_exp2f(s[r]);
            o = MFMA(v0, pack8(s, 0), o); o = MFMA(v1, pack8(s, 1), o);
        }
    }
#undef A_STAGE
    mref = m;
}

__global__ __launch_bounds__(1024) void attn_kernel(char* __restrict__ ws) {
    __shared__ __attribute__((aligned(16))) char smem[A_LDS];
    const lds_ptr lds = (lds_ptr)smem;
    const int tid = threadIdx.x, lane = tid & 63, w = __builtin_amdgcn_readfirstlane(tid >> 6);
    const int qb = w & 7, kh = w >> 3, h = lane >> 5;
    const int bid = blockIdx.x, xcd = bid & 7, idx = bid >> 3, slot = xcd + 8 * (idx >> 3), qblk = idx & 7;
    const int T = qblk * 8 + qb;
    const char* qsrc = ws + QF_OFF + ((size_t)(slot * 64 + T)) * 2048 + lane * 16;
    const bf16x8 q0 = *(const bf16x8*)qsrc, q1 = *(const bf16x8*)(qsrc + 1024);
    const char* kvsrc = ws + KV_OFF + ((size_t)(slot * 64 + kh * 32)) * 4096;
    f32x16 o = {}; float m;
    attn_loop<false>(kvsrc, lds, kh * A_GRP, qb, lane, q0, q1, o, m);
    volatile __attribute__((address_space(3))) int* flags = (volatile __attribute__((address_space(3))) int*)(lds + 2 * A_GRP);
    const bool bad = h && !(o[12] < 1.0e24f);
    const int anybad = __any(bad);
    if (lane == 0) flags[w] = anybad;
    BAR();
    int redo = 0;
#pragma unroll
    for (int i = 0; i < 16; ++i) redo |= flags[i];
    redo = __builtin_amdgcn_readfirstlane(redo);
    if (redo) {
        BAR();
#pragma unroll
        for (int r = 0; r < 16; ++r) o[r] = 0.f;
        attn_loop<true>(kvsrc, lds, kh * A_GRP, qb, lane, q0, q1, o, m);
        BAR();
    }
    __attribute__((address_space(3))) float* xo = (__attribute__((address_space(3))) float*)(lds + qb * 4352);
    if (kh) {
#pragma unroll
        for (int r = 0; r < 16; ++r) xo[r * 64 + lane] = o[r];
        xo[1024 + lane] = m;
    }
    BAR();
    if (!kh) {
        const float m2 = xo[1024 + lane], mn = fmaxf(m, m2), a1 = __builtin_amdgcn_exp2f(m - mn), a2 = __builtin_amdgcn_exp2f(m2 - mn);
#pragma unroll
        for (int r = 0; r < 16; ++r) o[r] = a1 * o[r] + a2 * xo[r * 64 + lane];
        const float lo = swap_halves(o[12]);
        const float l = h ? o[12] : lo, inv = 1.0f / l;
#pragma unroll
        for (int r = 0; r < 16; ++r) o[r] *= inv;
        if (h) {
#pragma unroll
            for (int r = 12; r < 16; ++r) o[r] = 0.f;
        }
        char* od = ws + OF_OFF + ((size_t)(((slot >> 4) * 64 + T) * 16 + (slot & 15))) * 2048 + lane * 16;
        *(bf16x8*)od = pack8(o, 0); *(bf16x8*)(od + 1024) = pack8(o, 1);
    }
}

__global__ __launch_bounds__(512) void oproj_kernel(const __hip_bfloat16* __restrict__ woA, const __hip_bfloat16* __restrict__ woB, const char* __restrict__ ws,
                                                    __hip_bfloat16* __restrict__ out) {
    __shared__ __attribute__((aligned(16))) char smem[448 * 128];
    const lds_ptr lds = (lds_ptr)smem;
    const int tid = threadIdx.x, lane = tid & 63, w = __builtin_amdgcn_readfirstlane(tid >> 6);
    const int bid = blockIdx.x, p = bid / 112, rem = bid % 112, mt = rem / 7, nb = rem % 7;
    const __hip_bfloat16* Wo = p ? woB : woA;
#pragma unroll
    for (int i = 0; i < 7; ++i) { const int ii = 7 * w + i, row = 8 * ii + (lane >> 3), pos = lane & 7, t = (pos >> 2) ^ ((row >> 1) & 1);
        glds16(Wo + (size_t)row * DM + nb * 64 + t * 32 + (pos & 3) * 8, lds + ii * 1024); }
    const int tl = w & 3, nt = w >> 2, h = lane >> 5, T = mt * 4 + tl;
    const char* osrc = ws + OF_OFF + ((size_t)((p * 64 + T) * 16)) * 2048 + lane * 16;
    bf16x8 of[32];
#pragma unroll
    for (int i = 0; i < 32; ++i) of[i] = *(const bf16x8*)(osrc + i * 1024);
    WAITV(0); BAR();
    const int q = (lane & 15) >> 2, cb = ((lane >> 4) & 1) * 32 + (lane & 3) * 8;
    f32x16 acc = {};
#pragma unroll
    for (int hd = 0; hd < 16; ++hd) {
#pragma unroll
        for (int s = 0; s < 2; ++s) {
            s16x4 f[2];
#pragma unroll
            for (int r = 0; r < 2; ++r) { int k = hd * 28 + 16 * s + 8 * r + 4 * h + q; if (hd == 15 && s == 1 && r == 1) k = min(k, 447);
                f[r] = ldstr(lds + k * 128 + ((nt ^ ((k >> 1) & 1)) * 64) + cb); }
            acc = MFMA(cat4(f[0], f[1]), of[hd * 2 + s], acc);
        }
    }
    __hip_bfloat16* od = out + (size_t)(T * 32 + (lane & 31)) * 896 + p * 448 + nb * 64 + nt * 32 + 4 * h;
#pragma unroll
    for (int g = 0; g < 4; ++g) { u32x2 v = {pk2(acc[4 * g], acc[4 * g + 1]), pk2(acc[4 * g + 2], acc[4 * g + 3])}; *(u32x2*)(od + 8 * g) = v; }
}

extern "C" void kernel_launch(void* const* d_in, const int* in_sizes, int n_in, void* d_out, int out_size, void* d_ws, size_t ws_size, hipStream_t stream) {
    const __hip_bfloat16* xA = (const __hip_bfloat16*)d_in[0]; const __hip_bfloat16* wA = (const __hip_bfloat16*)d_in[1]; const __hip_bfloat16* woA = (const __hip_bfloat16*)d_in[2];
    const __hip_bfloat16* xB = (const __hip_bfloat16*)d_in[3]; const __hip_bfloat16* wB = (const __hip_bfloat16*)d_in[4]; const __hip_bfloat16* woB = (const __hip_bfloat16*)d_in[5];
    char* ws = (char*)d_ws;
    qkv_kernel<<<256, 512, 0, stream>>>(xA, wA, xB, wB, ws);
    attn_kernel<<<256, 1024, 0, stream>>>(ws);
    oproj_kernel<<<224, 512, 0, stream>>>(woA, woB, ws, (__hip_bfloat16*)d_out);
}
```

```cpp
#include <hip/hip_runtime.h>
#include <hip/hip_bf16.h>
#include <stdint.h>

typedef __attribute__((ext_vector_type(8))) short bf16x8;
typedef __attribute__((ext_vector_type(4))) short s16x4;
typedef __attribute__((ext_vector_type(16))) float f32x16;
typedef __attribute__((ext_vector_type(4))) unsigned u32x4;
typedef __attribute__((ext_vector_type(2))) unsigned u32x2;
typedef __attribute__((ext_vector_type(2))) float f32x2;
typedef __attribute__((ext_vector_type(2))) __bf16 b16x2;
typedef __attribute__((address_space(3))) char* lds_ptr;
typedef __attribute__((address_space(3))) const char* lds_cptr;

#define DI __device__ __forceinline__
#define MFMA(a, b, c) __builtin_amdgcn_mfma_f32_32x32x16_bf16((a), (b), (c), 0, 0, 0)

constexpr int SEQ = 2048, DM = 448, NH = 16, DH = 28, N3 = 1344, NTILE = 64;
constexpr size_t QF_OFF = 0;
constexpr size_t KV_OFF = (size_t)4 << 20;
constexpr size_t OF_OFF = (size_t)12 << 20;
constexpr float QSCALE = 0.18898223650461362f * 1.4426950408889634f;

DI unsigned pk2(float a, float b) { f32x2 t = {a, b}; b16x2 r = __builtin_convertvector(t, b16x2); return __builtin_bit_cast(unsigned, r); }
DI bf16x8 pack8(const f32x16& x, int s) {
    u32x4 p = {pk2(x[8 * s], x[8 * s + 1]), pk2(x[8 * s + 2], x[8 * s + 3]), pk2(x[8 * s + 4], x[8 * s + 5]), pk2(x[8 * s + 6], x[8 * s + 7])};
    return __builtin_bit_cast(bf16x8, p);
}
DI void glds16(const void* g, lds_ptr l) {
    const unsigned base = __builtin_amdgcn_readfirstlane((unsigned)(uintptr_t)l); unsigned sv;
    asm volatile("s_mov_b32 %0, m0\n\ts_mov_b32 m0, %2\n\ts_nop 0\n\tglobal_load_lds_dwordx4 %1, off\n\ts_mov_b32 m0, %0" : "=&s"(sv) : "v"(g), "s"(base) : "memory");
}
DI bf16x8 ldsr16(lds_cptr p) { return *(const __attribute__((address_space(3))) bf16x8*)p; }
DI s16x4 ldstr(lds_cptr p) { return __builtin_bit_cast(s16x4, __builtin_amdgcn_ds_read_tr16_b64_v4i16((__attribute__((address_space(3))) s16x4*)p)); }
DI bf16x8 cat4(s16x4 a, s16x4 b) { return __builtin_shufflevector(a, b, 0, 1, 2, 3, 4, 5, 6, 7); }
#define STMODE 1
DI void st16(void* p, bf16x8 v) {
    asm volatile("global_store_dwordx4 %0, %1, off sc1\n\ts_nop 1" :: "v"(p), "v"(v) : "memory");
}
#define WAITV(n) asm volatile("s_waitcnt vmcnt(" #n ")" ::: "memory")
#define BAR() do { asm volatile("s_waitcnt lgkmcnt(0)" ::: "memory"); __builtin_amdgcn_s_barrier(); asm volatile("" ::: "memory"); } while (0)
DI float swap_halves(float v) {
    auto r = __builtin_amdgcn_permlane32_swap(__float_as_uint(v), __float_as_uint(v), false, false);
    return (threadIdx.x & 32) ? __uint_as_float(r[0]) : __uint_as_float(r[1]);
}

constexpr int G1_XB = 16384, G1_WB = 24576, G1_STG = G1_XB + G1_WB, G1_LDS = 3 * G1_STG;
__global__ __launch_bounds__(512) void qkv_kernel(const __hip_bfloat16* __restrict__ xA, const __hip_bfloat16* __restrict__ wA,
                                                  const __hip_bfloat16* __restrict__ xB, const __hip_bfloat16* __restrict__ wB, char* __restrict__ ws) {
    __shared__ __attribute__((aligned(16))) char smem[G1_LDS];
    const lds_ptr lds = (lds_ptr)smem;
    const int tid = threadIdx.x, lane = tid & 63, w = __builtin_amdgcn_readfirstlane(tid >> 6);
    const int bid = blockIdx.x, xcd = bid & 7, idx = bid >> 3;
    const int p = xcd >> 2, mt = 4 * (xcd & 3) + (idx >> 3), hj = idx & 7;
    const __hip_bfloat16* x = p ? xB : xA; const __hip_bfloat16* W = p ? wB : wA;
    const __hip_bfloat16* xsrc[2]; const __hip_bfloat16* wsrc[3];
#pragma unroll
    for (int i = 0; i < 2; ++i) { const int row = 8 * (2 * w + i) + (lane >> 3), c = (lane & 7) ^ ((row >> 1) & 7); xsrc[i] = x + (size_t)(mt * 128 + row) * DM + 8 * c; }
#pragma unroll
    for (int i = 0; i < 3; ++i) { int e = 64 * (3 * w + i) + lane; if (e >= 1344) e -= 1344; const int row = e / 21, cc = e % 21;
        wsrc[i] = W + (size_t)row * N3 + (cc / 7) * DM + hj * 56 + (cc % 7) * 8; }
#define G1_STAGE(kc, slot) do { _Pragma("unroll") for (int i = 0; i < 2; ++i) glds16(xsrc[i] + (kc) * 64, lds + (slot) * G1_STG + (2 * w + i) * 1024); \
        _Pragma("unroll") for (int i = 0; i < 3; ++i) glds16(wsrc[i] + (size_t)(kc) * 64 * N3, lds + (slot) * G1_STG + G1_XB + (3 * w + i) * 1024); } while (0)
    G1_STAGE(0, 0); G1_STAGE(1, 1);
    const int tl = w & 3, hs = w >> 2, h = lane >> 5, r32 = lane & 31;
    const int xrow = 32 * tl + r32, xsw = (xrow >> 1) & 7;
    const int wbase = (8 * h + ((lane & 15) >> 2)) * 336 + hs * 56 + ((lane >> 4) & 1) * 32 + (lane & 3) * 8;
    f32x16 aq = {}, ak = {}, av = {};
#pragma unroll
    for (int kc = 0; kc < 7; ++kc) {
        if (kc < 6) WAITV(5); else WAITV(0);
        BAR();
        if (kc + 2 < 7) G1_STAGE(kc + 2, (kc + 2) % 3);
        const lds_cptr xs = lds + (kc % 3) * G1_STG, wsb = xs + G1_XB + wbase;
#pragma unroll
        for (int ks = 0; ks < 4; ++ks) {
            const bf16x8 xf = ldsr16(xs + xrow * 128 + (((2 * ks + h) ^ xsw) * 16));
            const bf16x8 wq = cat4(ldstr(wsb + ks * 16 * 336), ldstr(wsb + (ks * 16 + 4) * 336));
            const bf16x8 wk = cat4(ldstr(wsb + ks * 16 * 336 + 112), ldstr(wsb + (ks * 16 + 4) * 336 + 112));
            const bf16x8 wv = cat4(ldstr(wsb + ks * 16 * 336 + 224), ldstr(wsb + (ks * 16 + 4) * 336 + 224));
            aq = MFMA(wq, xf, aq); ak = MFMA(wk, xf, ak); av = MFMA(xf, wv, av);
        }
    }
#undef G1_STAGE
    const int slot = p * 16 + 2 * hj + hs, T = mt * 4 + tl;
#pragma unroll
    for (int r = 0; r < 16; ++r) aq[r] *= QSCALE;
    if (h) {
#pragma unroll
        for (int r = 12; r < 16; ++r) { aq[r] = 0.f; ak[r] = 0.f; }
        ak[12] = 1.0f;
    }
    if (r32 >= 28) { const float f = (r32 == 28) ? 1.f : 0.f;
#pragma unroll
        for (int r = 0; r < 16; ++r) av[r] = f; }
    char* qd = ws + QF_OFF + ((size_t)(slot * 64 + T)) * 2048 + lane * 16;
    char* kd = ws + KV_OFF + ((size_t)(slot * 64 + T)) * 4096 + lane * 16;
    st16(qd, pack8(aq, 0)); st16(qd + 1024, pack8(aq, 1));
    st16(kd, pack8(ak, 0)); st16(kd + 1024, pack8(ak, 1));
    st16(kd + 2048, pack8(av, 0)); st16(kd + 3072, pack8(av, 1));
}

constexpr int A_CH = 16384, A_GRP = 4 * A_CH, A_LDS = 2 * A_GRP + 256;
DI float rowmax16(const f32x16& s) {
    float t = fmaxf(s[0], s[1]);
#pragma unroll
    for (int r = 2; r < 16; r += 2) t = fmaxf(fmaxf(t, s[r]), s[r + 1]);
    return fmaxf(t, swap_halves(t));
}
#define A_STAGE(c) do { _Pragma("unroll") for (int i_ = 0; i_ < 4; ++i_) glds16(src + (size_t)(c) * A_CH + i_ * 1024, dst + ((c) & 3) * A_CH + i_ * 1024); } while (0)
#define EXP16(s) do { _Pragma("unroll") for (int r_ = 0; r_ < 16; ++r_) s[r_] = __builtin_amdgcn_exp2f(s[r_]); } while (0)

DI void attn_safe(const char* __restrict__ src, lds_ptr dst, lds_cptr rd, bf16x8 qa0, bf16x8 qa1, bf16x8 qb0, bf16x8 qb1, f32x16& oA, f32x16& oB, float& mA, float& mB) {
    A_STAGE(0); A_STAGE(1); A_STAGE(2);
    oA = f32x16{}; oB = f32x16{}; mA = -INFINITY; mB = -INFINITY;
    for (int c = 0; c < 8; ++c) {
        if (c < 6) WAITV(8); else if (c == 6) WAITV(4); else WAITV(0);
        BAR();
        if (c + 3 < 8) A_STAGE(c + 3);
        const lds_cptr cb = rd + (c & 3) * A_CH;
        for (int t = 0; t < 4; ++t) {
            const bf16x8 k0 = ldsr16(cb + t * 4096), k1 = ldsr16(cb + t * 4096 + 1024), v0 = ldsr16(cb + t * 4096 + 2048), v1 = ldsr16(cb + t * 4096 + 3072);
            const f32x16 z = {};
            f32x16 s = MFMA(k0, qa0, z); s = MFMA(k1, qa1, s);
            float mn = fmaxf(mA, rowmax16(s)), al = __builtin_amdgcn_exp2f(mA - mn); mA = mn;
#pragma unroll
            for (int r = 0; r < 16; ++r) { oA[r] *= al; s[r] = __builtin_amdgcn_exp2f(s[r] - mn); }
            oA = MFMA(v0, pack8(s, 0), oA); oA = MFMA(v1, pack8(s, 1), oA);
            s = MFMA(k0, qb0, z); s = MFMA(k1, qb1, s);
            mn = fmaxf(mB, rowmax16(s)); al = __builtin_amdgcn_exp2f(mB - mn); mB = mn;
#pragma unroll
            for (int r = 0; r < 16; ++r) { oB[r] *= al; s[r] = __builtin_amdgcn_exp2f(s[r] - mn); }
            oB = MFMA(v0, pack8(s, 0), oB); oB = MFMA(v1, pack8(s, 1), oB);
        }
    }
}

__global__ __launch_bounds__(512) void attn_kernel(char* __restrict__ ws) {
    __shared__ __attribute__((aligned(16))) char smem[A_LDS];
    const lds_ptr lds = (lds_ptr)smem;
    const int tid = threadIdx.x, lane = tid & 63, w = __builtin_amdgcn_readfirstlane(tid >> 6);
    const int qg = w & 3, kh = w >> 2, h = lane >> 5;
    const int bid = blockIdx.x, xcd = bid & 7, idx = bid >> 3, slot = xcd + 8 * (idx >> 3), qblk = idx & 7;
    const int T0 = qblk * 8 + 2 * qg;
    const char* src = ws + KV_OFF + ((size_t)(slot * 64 + kh * 32)) * 4096 + qg * 4096 + lane * 16;
    const lds_ptr dst = lds + kh * A_GRP + qg * 4096;
    const char* qsrc = ws + QF_OFF + ((size_t)(slot * 64 + T0)) * 2048 + lane * 16;
    bf16x8 qA0, qA1, qB0, qB1;
    asm volatile("global_load_dwordx4 %0, %4, off\n\tglobal_load_dwordx4 %1, %4, off offset:1024\n\tglobal_load_dwordx4 %2, %4, off offset:2048\n\tglobal_load_dwordx4 %3, %4, off offset:3072"
                 : "=&v"(qA0), "=&v"(qA1), "=&v"(qB0), "=&v"(qB1) : "v"(qsrc) : "memory");
    A_STAGE(0);
    const lds_cptr rd = lds + kh * A_GRP + lane * 16;
    const f32x16 z = {};
    f32x16 oA = {}, oB = {}, sA, sB, sAn, sBn;
    float mA, mB;
    asm volatile("s_waitcnt vmcnt(0)" : "+v"(qA0), "+v"(qA1), "+v"(qB0), "+v"(qB1) :: "memory");
    BAR();
    A_STAGE(1); A_STAGE(2);
    bf16x8 k0 = ldsr16(rd), k1 = ldsr16(rd + 1024), v0, v1, v0n, v1n;
    {
        sA = MFMA(k0, qA0, z); sA = MFMA(k1, qA1, sA); sB = MFMA(k0, qB0, z); sB = MFMA(k1, qB1, sB);
        const unsigned pm = pk2(-rowmax16(sA), -rowmax16(sB));
        mA = -__uint_as_float(pm << 16); mB = -__uint_as_float(pm & 0xffff0000u);
#pragma unroll
        for (int r = 0; r < 16; ++r) { sA[r] -= mA; sB[r] -= mB; }
    }
    bf16x8 qA1p = qA1, qB1p = qB1;
    if (h) { qA1p[4] = (short)(__float_as_uint(-mA) >> 16); qB1p[4] = (short)(__float_as_uint(-mB) >> 16); }
    v0 = ldsr16(rd + 2048); v1 = ldsr16(rd + 3072);
    k0 = ldsr16(rd + 4096); k1 = ldsr16(rd + 4096 + 1024);
    for (int c = 0; c < 8; ++c) {
        const lds_cptr cb = rd + (c & 3) * A_CH, cn = rd + ((c + 1) & 3) * A_CH;
#pragma unroll
        for (int t = 0; t < 4; ++t) {
            const bool last = (c == 7 && t == 3);
            if (t == 2 && c < 7) {
                if (c < 6) WAITV(4); else WAITV(0);
                BAR();
                if (c + 3 < 8) A_STAGE(c + 3);
            }
            if (!last) { sAn = MFMA(k0, qA0, z); sAn = MFMA(k1, qA1p, sAn); sBn = MFMA(k0, qB0, z); sBn = MFMA(k1, qB1p, sBn); }
            if (!last) { const lds_cptr vp = (t < 3) ? cb + (t + 1) * 4096 : cn; v0n = ldsr16(vp + 2048); v1n = ldsr16(vp + 3072); }
            if (!(c == 7 && t >= 2)) { const lds_cptr kp = (t < 2) ? cb + (t + 2) * 4096 : cn + (t - 2) * 4096; k0 = ldsr16(kp); k1 = ldsr16(kp + 1024); }
            EXP16(sA);
            oA = MFMA(v0, pack8(sA, 0), oA); oA = MFMA(v1, pack8(sA, 1), oA);
            EXP16(sB);
            oB = MFMA(v0, pack8(sB, 0), oB); oB = MFMA(v1, pack8(sB, 1), oB);
            sA = sAn; sB = sBn; v0 = v0n; v1 = v1n;
        }
    }
    __attribute__((address_space(3))) int* flags = (__attribute__((address_space(3))) int*)(lds + 2 * A_GRP);
    const bool bad = h && !(fmaxf(oA[12], oB[12]) < 1.0e24f && oA[12] == oA[12] && oB[12] == oB[12]);
    const int anybad = __any(bad);
    if (lane == 0) flags[w] = anybad;
    BAR();
    typedef __attribute__((ext_vector_type(4))) float f32x4;
    __attribute__((address_space(3))) f32x4* xw = (__attribute__((address_space(3))) f32x4*)(lds + (qg * 2 + kh) * 4608);
    const __attribute__((address_space(3))) f32x4* xr = (const __attribute__((address_space(3))) f32x4*)(lds + (qg * 2 + (kh ^ 1)) * 4608);
#define A_PUT() do { const f32x16& og = kh ? oA : oB; _Pragma("unroll") for (int g = 0; g < 4; ++g) xw[g * 64 + lane] = f32x4{og[4 * g], og[4 * g + 1], og[4 * g + 2], og[4 * g + 3]}; \
        ((__attribute__((address_space(3))) float*)(xw + 256))[lane] = kh ? mA : mB; } while (0)
    A_PUT();
    const int redo = __any(flags[lane & 7]);
    BAR();
    if (redo) {
        BAR();
        attn_safe(src, dst, rd, qA0, qA1, qB0, qB1, oA, oB, mA, mB);
        BAR();
        A_PUT();
        BAR();
    }
    {
        f32x16 o = kh ? oB : oA; const float m = kh ? mB : mA;
        const float m2 = ((const __attribute__((address_space(3))) float*)(xr + 256))[lane], mn = fmaxf(m, m2), a1 = __builtin_amdgcn_exp2f(m - mn), a2 = __builtin_amdgcn_exp2f(m2 - mn);
#pragma unroll
        for (int g = 0; g < 4; ++g) { const f32x4 t = xr[g * 64 + lane];
#pragma unroll
            for (int j = 0; j < 4; ++j) o[4 * g + j] = a1 * o[4 * g + j] + a2 * t[j]; }
        const float lo = swap_halves(o[12]);
        const float l = h ? o[12] : lo, inv = 1.0f / l;
#pragma unroll
        for (int r = 0; r < 16; ++r) o[r] *= inv;
        if (h) {
#pragma unroll
            for (int r = 12; r < 16; ++r) o[r] = 0.f;
        }
        char* od = ws + OF_OFF + ((size_t)(((slot >> 4) * 64 + T0 + kh) * 16 + (slot & 15))) * 2048 + lane * 16;
        st16(od, pack8(o, 0)); st16(od + 1024, pack8(o, 1));
    }
}

__global__ __launch_bounds__(512) void oproj_kernel(const __hip_bfloat16* __restrict__ woA, const __hip_bfloat16* __restrict__ woB, const char* __restrict__ ws,
                                                    __hip_bfloat16* __restrict__ out) {
    __shared__ __attribute__((aligned(16))) char smem[448 * 128];
    const lds_ptr lds = (lds_ptr)smem;
    const int tid = threadIdx.x, lane = tid & 63, w = __builtin_amdgcn_readfirstlane(tid >> 6);
    const int bid = blockIdx.x, xcd = bid & 7, idx = bid >> 3, p = xcd >> 2, mt = 4 * (xcd & 3) + idx / 7, nb = idx % 7;
    const __hip_bfloat16* Wo = p ? woB : woA;
#pragma unroll
    for (int i = 0; i < 7; ++i) { const int ii = 7 * w + i, row = 8 * ii + (lane >> 3), pos = lane & 7, t = (pos >> 2) ^ ((row >> 1) & 1);
        glds16(Wo + (size_t)row * DM + nb * 64 + t * 32 + (pos & 3) * 8, lds + ii * 1024); }
    const int tl = w & 3, nt = w >> 2, h = lane >> 5, T = mt * 4 + tl;
    const char* osrc = ws + OF_OFF + ((size_t)((p * 64 + T) * 16)) * 2048 + lane * 16;
    bf16x8 of[32];
#pragma unroll
    for (int i = 0; i < 32; ++i) of[i] = *(const bf16x8*)(osrc + i * 1024);
    WAITV(0); BAR();
    const int q = (lane & 15) >> 2, cb = ((lane >> 4) & 1) * 32 + (lane & 3) * 8;
    f32x16 acc = {}, acc2 = {};
#pragma unroll
    for (int hd = 0; hd < 16; ++hd) {
#pragma unroll
        for (int s = 0; s < 2; ++s) {
            s16x4 f[2];
#pragma unroll
            for (int r = 0; r < 2; ++r) { int k = hd * 28 + 16 * s + 8 * r + 4 * h + q; if (hd == 15 && s == 1 && r == 1) k = min(k, 447);
                f[r] = ldstr(lds + k * 128 + ((nt ^ ((k >> 1) & 1)) * 64) + cb); }
            if (s) acc2 = MFMA(cat4(f[0], f[1]), of[hd * 2 + s], acc2); else acc = MFMA(cat4(f[0], f[1]), of[hd * 2 + s], acc);
        }
    }
#pragma unroll
    for (int r = 0; r < 16; ++r) acc[r] += acc2[r];
    __hip_bfloat16* od = out + (size_t)(T * 32 + (lane & 31)) * 896 + p * 448 + nb * 64 + nt * 32 + 4 * h;
#pragma unroll
    for (int g = 0; g < 4; ++g) { u32x2 v = {pk2(acc[4 * g], acc[4 * g + 1]), pk2(acc[4 * g + 2], acc[4 * g + 3])}; *(u32x2*)(od + 8 * g) = v; }
}

extern "C" void kernel_launch(void* const* d_in, const int* in_sizes, int n_in, void* d_out, int out_size, void* d_ws, size_t ws_size, hipStream_t stream) {
    const __hip_bfloat16* xA = (const __hip_bfloat16*)d_in[0]; const __hip_bfloat16* wA = (const __hip_bfloat16*)d_in[1]; const __hip_bfloat16* woA = (const __hip_bfloat16*)d_in[2];
    const __hip_bfloat16* xB = (const __hip_bfloat16*)d_in[3]; const __hip_bfloat16* wB = (const __hip_bfloat16*)d_in[4]; const __hip_bfloat16* woB = (const __hip_bfloat16*)d_in[5];
    char* ws = (char*)d_ws;
    qkv_kernel<<<256, 512, 0, stream>>>(xA, wA, xB, wB, ws);
    attn_kernel<<<256, 512, 0, stream>>>(ws);
    oproj_kernel<<<224, 512, 0, stream>>>(woA, woB, ws, (__hip_bfloat16*)d_out);
}
```

```cpp
#include <hip/hip_runtime.h>
#include <hip/hip_bf16.h>
#include <stdint.h>

typedef __attribute__((ext_vector_type(8))) short bf16x8;
typedef __attribute__((ext_vector_type(4))) short s16x4;
typedef __attribute__((ext_vector_type(16))) float f32x16;
typedef __attribute__((ext_vector_type(4))) unsigned u32x4;
typedef __attribute__((ext_vector_type(2))) unsigned u32x2;
typedef __attribute__((ext_vector_type(2))) float f32x2;
typedef __attribute__((ext_vector_type(2))) __bf16 b16x2;
typedef __attribute__((address_space(3))) char* lds_ptr;
typedef __attribute__((address_space(3))) const char* lds_cptr;

#define DI __device__ __forceinline__
#define MFMA(a, b, c) __builtin_amdgcn_mfma_f32_32x32x16_bf16((a), (b), (c), 0, 0, 0)

constexpr int SEQ = 2048, DM = 448, NH = 16, DH = 28, N3 = 1344, NTILE = 64;
constexpr size_t QF_OFF = 0;
constexpr size_t KV_OFF = (size_t)4 << 20;
constexpr size_t OF_OFF = (size_t)12 << 20;
constexpr size_t FLAG_OFF = (size_t)16 << 20;
extern "C" __device__ unsigned long long hw_dispatch_id() __asm("llvm.amdgcn.dispatch.id");
constexpr float QSCALE = 0.18898223650461362f * 1.4426950408889634f;

DI unsigned pk2(float a, float b) { f32x2 t = {a, b}; b16x2 r = __builtin_convertvector(t, b16x2); return __builtin_bit_cast(unsigned, r); }
DI bf16x8 pack8(const f32x16& x, int s) {
    u32x4 p = {pk2(x[8 * s], x[8 * s + 1]), pk2(x[8 * s + 2], x[8 * s + 3]), pk2(x[8 * s + 4], x[8 * s + 5]), pk2(x[8 * s + 6], x[8 * s + 7])};
    return __builtin_bit_cast(bf16x8, p);
}
DI void glds16(const void* g, lds_ptr l) {
    const unsigned base = __builtin_amdgcn_readfirstlane((unsigned)(uintptr_t)l); unsigned sv;
    asm volatile("s_mov_b32 %0, m0\n\ts_mov_b32 m0, %2\n\ts_nop 0\n\tglobal_load_lds_dwordx4 %1, off\n\ts_mov_b32 m0, %0" : "=&s"(sv) : "v"(g), "s"(base) : "memory");
}
DI void glds16c(const void* g, lds_ptr l) {
    const unsigned base = __builtin_amdgcn_readfirstlane((unsigned)(uintptr_t)l); unsigned sv;
    asm volatile("s_mov_b32 %0, m0\n\ts_mov_b32 m0, %2\n\ts_nop 0\n\tglobal_load_lds_dwordx4 %1, off sc1\n\ts_mov_b32 m0, %0" : "=&s"(sv) : "v"(g), "s"(base) : "memory");
}
DI bf16x8 ldsr16(lds_cptr p) { return *(const __attribute__((address_space(3))) bf16x8*)p; }
DI s16x4 ldstr(lds_cptr p) { return __builtin_bit_cast(s16x4, __builtin_amdgcn_ds_read_tr16_b64_v4i16((__attribute__((address_space(3))) s16x4*)p)); }
DI bf16x8 cat4(s16x4 a, s16x4 b) { return __builtin_shufflevector(a, b, 0, 1, 2, 3, 4, 5, 6, 7); }
#define STMODE 1
DI void st16(void* p, bf16x8 v) {
    asm volatile("global_store_dwordx4 %0, %1, off sc1\n\ts_nop 1" :: "v"(p), "v"(v) : "memory");
}
#define WAITV(n) asm volatile("s_waitcnt vmcnt(" #n ")" ::: "memory")
#define BAR() do { asm volatile("s_waitcnt lgkmcnt(0)" ::: "memory"); __builtin_amdgcn_s_barrier(); asm volatile("" ::: "memory"); } while (0)
DI float swap_halves(float v) {
    auto r = __builtin_amdgcn_permlane32_swap(__float_as_uint(v), __float_as_uint(v), false, false);
    return (threadIdx.x & 32) ? __uint_as_float(r[0]) : __uint_as_float(r[1]);
}

constexpr int F_XB = 32768, F_WB = 12288, F_STG = F_XB + F_WB;
constexpr int A_CH = 16384, A_GRP = 4 * A_CH, F_LDS = 3 * F_STG + 256, F_FLAGS = 3 * F_STG;
DI float rowmax16(const f32x16& s) {
    float t = fmaxf(s[0], s[1]);
#pragma unroll
    for (int r = 2; r < 16; r += 2) t = fmaxf(fmaxf(t, s[r]), s[r + 1]);
    return fmaxf(t, swap_halves(t));
}
#define A_STAGE(c) do { const char* s_ = kvh + (size_t)((((qblk + (c)) & 7) * 8) * 4096); _Pragma("unroll") for (int i_ = 0; i_ < 4; ++i_) glds16c(s_ + i_ * 1024, dst + ((c) & 3) * A_CH + i_ * 1024); } while (0)
#define EXP16(s) do { _Pragma("unroll") for (int r_ = 0; r_ < 16; ++r_) s[r_] = __builtin_amdgcn_exp2f(s[r_]); } while (0)

DI void attn_safe(const char* __restrict__ kvh, int qblk, lds_ptr dst, lds_cptr rd, bf16x8 qa0, bf16x8 qa1, bf16x8 qb0, bf16x8 qb1, f32x16& oA, f32x16& oB, float& mA, float& mB) {
    A_STAGE(0); A_STAGE(1); A_STAGE(2);
    oA = f32x16{}; oB = f32x16{}; mA = -INFINITY; mB = -INFINITY;
    for (int c = 0; c < 8; ++c) {
        if (c < 6) WAITV(8); else if (c == 6) WAITV(4); else WAITV(0);
        BAR();
        if (c + 3 < 8) A_STAGE(c + 3);
        const lds_cptr cb = rd + (c & 3) * A_CH;
        for (int t = 0; t < 4; ++t) {
            const bf16x8 k0 = ldsr16(cb + t * 4096), k1 = ldsr16(cb + t * 4096 + 1024), v0 = ldsr16(cb + t * 4096 + 2048), v1 = ldsr16(cb + t * 4096 + 3072);
            const f32x16 z = {};
            f32x16 s = MFMA(k0, qa0, z); s = MFMA(k1, qa1, s);
            float mn = fmaxf(mA, rowmax16(s)), al = __builtin_amdgcn_exp2f(mA - mn); mA = mn;
#pragma unroll
            for (int r = 0; r < 16; ++r) { oA[r] *= al; s[r] = __builtin_amdgcn_exp2f(s[r] - mn); }
            oA = MFMA(v0, pack8(s, 0), oA); oA = MFMA(v1, pack8(s, 1), oA);
            s = MFMA(k0, qb0, z); s = MFMA(k1, qb1, s);
            mn = fmaxf(mB, rowmax16(s)); al = __builtin_amdgcn_exp2f(mB - mn); mB = mn;
#pragma unroll
            for (int r = 0; r < 16; ++r) { oB[r] *= al; s[r] = __builtin_amdgcn_exp2f(s[r] - mn); }
            oB = MFMA(v0, pack8(s, 0), oB); oB = MFMA(v1, pack8(s, 1), oB);
        }
    }
}

__global__ __launch_bounds__(512) void fused_kernel(const __hip_bfloat16* __restrict__ xA, const __hip_bfloat16* __restrict__ wA,
                                                    const __hip_bfloat16* __restrict__ xB, const __hip_bfloat16* __restrict__ wB, char* __restrict__ ws) {
    __shared__ __attribute__((aligned(16))) char smem[F_LDS];
    const lds_ptr lds = (lds_ptr)smem;
    const int tid = threadIdx.x, lane = tid & 63, w = __builtin_amdgcn_readfirstlane(tid >> 6);
    const int h = lane >> 5, r32 = lane & 31;
    const int bid = blockIdx.x, xcd = bid & 7, idx = bid >> 3, slot = xcd + 8 * (idx >> 3), qblk = idx & 7;
    const int head = slot & 15;
    {
        const __hip_bfloat16* x = (slot >> 4) ? xB : xA; const __hip_bfloat16* W = (slot >> 4) ? wB : wA;
        const __hip_bfloat16* xsrc[4]; const __hip_bfloat16* wsrc[2];
#pragma unroll
        for (int i = 0; i < 4; ++i) { const int row = 32 * w + 8 * i + (lane >> 3), c = (lane & 7) ^ ((row >> 1) & 7); xsrc[i] = x + (size_t)(qblk * 256 + row) * DM + 8 * c; }
        const int j0 = (w < 4) ? 2 * w : 4 + w;
#pragma unroll
        for (int i = 0; i < 2; ++i) { const int j = j0 + i, row = 16 * (j & 3) + (lane >> 2), pc = lane & 3;
            wsrc[i] = W + (size_t)row * N3 + (j >> 2) * DM + head * DH + (pc == 3 ? 20 : 8 * pc); }
#define F_STAGE(kc, sl) do { _Pragma("unroll") for (int i = 0; i < 4; ++i) glds16(xsrc[i] + (kc) * 64, lds + (sl) * F_STG + (4 * w + i) * 1024); \
        glds16(wsrc[0] + (size_t)(kc) * 64 * N3, lds + (sl) * F_STG + F_XB + j0 * 1024); \
        if (w < 4) glds16(wsrc[1] + (size_t)(kc) * 64 * N3, lds + (sl) * F_STG + F_XB + (j0 + 1) * 1024); } while (0)
        F_STAGE(0, 0); F_STAGE(1, 1);
        const int xrow = 32 * w + r32, xsw = (xrow >> 1) & 7, col4 = ((lane >> 4) & 1) * 4 + (lane & 3);
        const int wbase = F_XB + (8 * h + ((lane & 15) >> 2)) * 64 + (col4 <= 5 ? col4 * 8 : 56);
        f32x16 aq = {}, ak = {}, av = {};
#pragma unroll
        for (int kc = 0; kc < 7; ++kc) {
            if (kc < 6) { if (w < 4) WAITV(6); else WAITV(5); } else WAITV(0);
            BAR();
            if (kc + 2 < 7) F_STAGE(kc + 2, (kc + 2) % 3);
            const lds_cptr xs = lds + (kc % 3) * F_STG, wsb = xs + wbase;
#pragma unroll
            for (int ks = 0; ks < 4; ++ks) {
                const bf16x8 xf = ldsr16(xs + xrow * 128 + (((2 * ks + h) ^ xsw) * 16));
                const bf16x8 wq = cat4(ldstr(wsb + ks * 1024), ldstr(wsb + ks * 1024 + 256));
                const bf16x8 wk = cat4(ldstr(wsb + ks * 1024 + 4096), ldstr(wsb + ks * 1024 + 4096 + 256));
                const bf16x8 wv = cat4(ldstr(wsb + ks * 1024 + 8192), ldstr(wsb + ks * 1024 + 8192 + 256));
                aq = MFMA(wq, xf, aq); ak = MFMA(wk, xf, ak); av = MFMA(xf, wv, av);
            }
        }
#undef F_STAGE
#pragma unroll
        for (int r = 0; r < 16; ++r) aq[r] *= QSCALE;
        if (h) {
#pragma unroll
            for (int r = 12; r < 16; ++r) { aq[r] = 0.f; ak[r] = 0.f; }
            ak[12] = 1.0f;
        }
        if (r32 >= 28) { const float f = (r32 == 28) ? 1.f : 0.f;
#pragma unroll
            for (int r = 0; r < 16; ++r) av[r] = f; }
        const bf16x8 fk0 = pack8(ak, 0), fk1 = pack8(ak, 1), fv0 = pack8(av, 0), fv1 = pack8(av, 1);
        char* kd = ws + KV_OFF + ((size_t)(slot * 64 + qblk * 8 + w)) * 4096 + lane * 16;
        st16(kd, fk0); st16(kd + 1024, fk1); st16(kd + 2048, fv0); st16(kd + 3072, fv1);
        BAR();
        typedef __attribute__((address_space(3))) bf16x8* lds_frag;
        lds_frag kl = (lds_frag)(lds + (w >> 2) * A_GRP + (w & 3) * 4096 + lane * 16);
        kl[0] = fk0; kl[64] = fk1; kl[128] = fv0; kl[192] = fv1;
        lds_frag ql = (lds_frag)(lds + 3 * A_CH + w * 2048 + lane * 16);
        ql[0] = pack8(aq, 0); ql[64] = pack8(aq, 1);
        BAR();
    }
    const int qg = w & 3, kh = w >> 2, T0 = qblk * 8 + 2 * qg;
    const char* kvh = ws + KV_OFF + ((size_t)(slot * 64 + kh * 4 + qg)) * 4096 + lane * 16;
    const lds_ptr dst = lds + kh * A_GRP + qg * 4096;
    const lds_cptr rd = lds + kh * A_GRP + lane * 16;
    const bf16x8 qA0 = ldsr16(lds + 3 * A_CH + qg * 4096 + lane * 16), qA1 = ldsr16(lds + 3 * A_CH + qg * 4096 + 1024 + lane * 16);
    const bf16x8 qB0 = ldsr16(lds + 3 * A_CH + qg * 4096 + 2048 + lane * 16), qB1 = ldsr16(lds + 3 * A_CH + qg * 4096 + 3072 + lane * 16);
    const f32x16 z = {};
    f32x16 oA = {}, oB = {}, sA, sB, sAn, sBn;
    float mA, mB;
    bf16x8 k0 = ldsr16(rd), k1 = ldsr16(rd + 1024), v0, v1, v0n, v1n;
    {
        sA = MFMA(k0, qA0, z); sA = MFMA(k1, qA1, sA); sB = MFMA(k0, qB0, z); sB = MFMA(k1, qB1, sB);
        const unsigned pm = pk2(-rowmax16(sA), -rowmax16(sB));
        mA = -__uint_as_float(pm << 16); mB = -__uint_as_float(pm & 0xffff0000u);
#pragma unroll
        for (int r = 0; r < 16; ++r) { sA[r] -= mA; sB[r] -= mB; }
    }
    bf16x8 qA1p = qA1, qB1p = qB1;
    if (h) { qA1p[4] = (short)(__float_as_uint(-mA) >> 16); qB1p[4] = (short)(__float_as_uint(-mB) >> 16); }
    const unsigned long long token = (hw_dispatch_id() * 0x9E3779B97F4A7C15ull) ^ ((unsigned long long)(uintptr_t)__builtin_amdgcn_dispatch_ptr() << 1) ^ 0x5851F42D4C957F2Dull;
    unsigned long long* flags_g = (unsigned long long*)(ws + FLAG_OFF) + slot * 8;
#pragma unroll
    for (int t = 0; t < 4; ++t) {
        if (t == 2) {
            WAITV(0); BAR();
            if (tid == 0) __hip_atomic_store(flags_g + qblk, token, __ATOMIC_RELAXED, __HIP_MEMORY_SCOPE_AGENT);
        }
        if (t == 3) {
            for (unsigned spins = 0; spins < (1u << 22); ++spins) {
                const unsigned long long f = __hip_atomic_load(flags_g + (lane & 7), __ATOMIC_RELAXED, __HIP_MEMORY_SCOPE_AGENT);
                if (__all(f == token)) break;
                __builtin_amdgcn_s_sleep(2);
            }
            A_STAGE(1); A_STAGE(2); A_STAGE(3);
        }
        if (t > 0) { k0 = ldsr16(rd + t * 4096); k1 = ldsr16(rd + t * 4096 + 1024);
            sA = MFMA(k0, qA0, z); sA = MFMA(k1, qA1p, sA); sB = MFMA(k0, qB0, z); sB = MFMA(k1, qB1p, sB); }
        v0 = ldsr16(rd + t * 4096 + 2048); v1 = ldsr16(rd + t * 4096 + 3072);
        EXP16(sA);
        oA = MFMA(v0, pack8(sA, 0), oA); oA = MFMA(v1, pack8(sA, 1), oA);
        EXP16(sB);
        oB = MFMA(v0, pack8(sB, 0), oB); oB = MFMA(v1, pack8(sB, 1), oB);
    }
    WAITV(8); BAR();
    k0 = ldsr16(rd + A_CH); k1 = ldsr16(rd + A_CH + 1024);
    sA = MFMA(k0, qA0, z); sA = MFMA(k1, qA1p, sA); sB = MFMA(k0, qB0, z); sB = MFMA(k1, qB1p, sB);
    v0 = ldsr16(rd + A_CH + 2048); v1 = ldsr16(rd + A_CH + 3072);
    k0 = ldsr16(rd + A_CH + 4096); k1 = ldsr16(rd + A_CH + 4096 + 1024);
    for (int c = 1; c < 8; ++c) {
        const lds_cptr cb = rd + (c & 3) * A_CH, cn = rd + ((c + 1) & 3) * A_CH;
#pragma unroll
        for (int t = 0; t < 4; ++t) {
            const bool last = (c == 7 && t == 3);
            if (t == 2 && c < 7) {
                if (c < 6) WAITV(4); else WAITV(0);
                BAR();
                if (c + 3 < 8) A_STAGE(c + 3);
            }
            if (!last) { sAn = MFMA(k0, qA0, z); sAn = MFMA(k1, qA1p, sAn); sBn = MFMA(k0, qB0, z); sBn = MFMA(k1, qB1p, sBn); }
            if (!last) { const lds_cptr vp = (t < 3) ? cb + (t + 1) * 4096 : cn; v0n = ldsr16(vp + 2048); v1n = ldsr16(vp + 3072); }
            if (!(c == 7 && t >= 2)) { const lds_cptr kp = (t < 2) ? cb + (t + 2) * 4096 : cn + (t - 2) * 4096; k0 = ldsr16(kp); k1 = ldsr16(kp + 1024); }
            EXP16(sA);
            oA = MFMA(v0, pack8(sA, 0), oA); oA = MFMA(v1, pack8(sA, 1), oA);
            EXP16(sB);
            oB = MFMA(v0, pack8(sB, 0), oB); oB = MFMA(v1, pack8(sB, 1), oB);
            sA = sAn; sB = sBn; v0 = v0n; v1 = v1n;
        }
    }
    __attribute__((address_space(3))) int* flags = (__attribute__((address_space(3))) int*)(lds + F_FLAGS);
    const bool bad = h && !(fmaxf(oA[12], oB[12]) < 1.0e24f && oA[12] == oA[12] && oB[12] == oB[12]);
    const int anybad = __any(bad);
    if (lane == 0) flags[w] = anybad;
    BAR();
    typedef __attribute__((ext_vector_type(4))) float f32x4;
    __attribute__((address_space(3))) f32x4* xw = (__attribute__((address_space(3))) f32x4*)(lds + (qg * 2 + kh) * 4608);
    const __attribute__((address_space(3))) f32x4* xr = (const __attribute__((address_space(3))) f32x4*)(lds + (qg * 2 + (kh ^ 1)) * 4608);
#define A_PUT() do { const f32x16& og = kh ? oA : oB; _Pragma("unroll") for (int g = 0; g < 4; ++g) xw[g * 64 + lane] = f32x4{og[4 * g], og[4 * g + 1], og[4 * g + 2], og[4 * g + 3]}; \
        ((__attribute__((address_space(3))) float*)(xw + 256))[lane] = kh ? mA : mB; } while (0)
    A_PUT();
    const int redo = __any(flags[lane & 7]);
    BAR();
    if (redo) {
        BAR();
        attn_safe(kvh, qblk, dst, rd, qA0, qA1, qB0, qB1, oA, oB, mA, mB);
        BAR();
        A_PUT();
        BAR();
    }
    {
        f32x16 o = kh ? oB : oA; const float m = kh ? mB : mA;
        const float m2 = ((const __attribute__((address_space(3))) float*)(xr + 256))[lane], mn = fmaxf(m, m2), a1 = __builtin_amdgcn_exp2f(m - mn), a2 = __builtin_amdgcn_exp2f(m2 - mn);
#pragma unroll
        for (int g = 0; g < 4; ++g) { const f32x4 t = xr[g * 64 + lane];
#pragma unroll
            for (int j = 0; j < 4; ++j) o[4 * g + j] = a1 * o[4 * g + j] + a2 * t[j]; }
        const float lo = swap_halves(o[12]);
        const float l = h ? o[12] : lo, inv = 1.0f / l;
#pragma unroll
        for (int r = 0; r < 16; ++r) o[r] *= inv;
        if (h) {
#pragma unroll
            for (int r = 12; r < 16; ++r) o[r] = 0.f;
        }
        char* od = ws + OF_OFF + ((size_t)(((slot >> 4) * 64 + T0 + kh) * 16 + (slot & 15))) * 2048 + lane * 16;
        st16(od, pack8(o, 0)); st16(od + 1024, pack8(o, 1));
    }
}

__global__ __launch_bounds__(512) void oproj_kernel(const __hip_bfloat16* __restrict__ woA, const __hip_bfloat16* __restrict__ woB, const char* __restrict__ ws,
                                                    __hip_bfloat16* __restrict__ out) {
    __shared__ __attribute__((aligned(16))) char smem[448 * 128];
    const lds_ptr lds = (lds_ptr)smem;
    const int tid = threadIdx.x, lane = tid & 63, w = __builtin_amdgcn_readfirstlane(tid >> 6);
    const int bid = blockIdx.x, xcd = bid & 7, idx = bid >> 3, p = xcd >> 2, mt = 4 * (xcd & 3) + idx / 7, nb = idx % 7;
    const __hip_bfloat16* Wo = p ? woB : woA;
#pragma unroll
    for (int i = 0; i < 7; ++i) { const int ii = 7 * w + i, row = 8 * ii + (lane >> 3), pos = lane & 7, t = (pos >> 2) ^ ((row >> 1) & 1);
        glds16(Wo + (size_t)row * DM + nb * 64 + t * 32 + (pos & 3) * 8, lds + ii * 1024); }
    const int tl = w & 3, nt = w >> 2, h = lane >> 5, T = mt * 4 + tl;
    const char* osrc = ws + OF_OFF + ((size_t)((p * 64 + T) * 16)) * 2048 + lane * 16;
    bf16x8 of[32];
#pragma unroll
    for (int i = 0; i < 32; ++i) of[i] = *(const bf16x8*)(osrc + i * 1024);
    WAITV(0); BAR();
    const int q = (lane & 15) >> 2, cb = ((lane >> 4) & 1) * 32 + (lane & 3) * 8;
    f32x16 acc = {}, acc2 = {};
#pragma unroll
    for (int hd = 0; hd < 16; ++hd) {
#pragma unroll
        for (int s = 0; s < 2; ++s) {
            s16x4 f[2];
#pragma unroll
            for (int r = 0; r < 2; ++r) { int k = hd * 28 + 16 * s + 8 * r + 4 * h + q; if (hd == 15 && s == 1 && r == 1) k = min(k, 447);
                f[r] = ldstr(lds + k * 128 + ((nt ^ ((k >> 1) & 1)) * 64) + cb); }
            if (s) acc2 = MFMA(cat4(f[0], f[1]), of[hd * 2 + s], acc2); else acc = MFMA(cat4(f[0], f[1]), of[hd * 2 + s], acc);
        }
    }
#pragma unroll
    for (int r = 0; r < 16; ++r) acc[r] += acc2[r];
    __hip_bfloat16* od = out + (size_t)(T * 32 + (lane & 31)) * 896 + p * 448 + nb * 64 + nt * 32 + 4 * h;
#pragma unroll
    for (int g = 0; g < 4; ++g) { u32x2 v = {pk2(acc[4 * g], acc[4 * g + 1]), pk2(acc[4 * g + 2], acc[4 * g + 3])}; *(u32x2*)(od + 8 * g) = v; }
}

extern "C" void kernel_launch(void* const* d_in, const int* in_sizes, int n_in, void* d_out, int out_size, void* d_ws, size_t ws_size, hipStream_t stream) {
    const __hip_bfloat16* xA = (const __hip_bfloat16*)d_in[0]; const __hip_bfloat16* wA = (const __hip_bfloat16*)d_in[1]; const __hip_bfloat16* woA = (const __hip_bfloat16*)d_in[2];
    const __hip_bfloat16* xB = (const __hip_bfloat16*)d_in[3]; const __hip_bfloat16* wB = (const __hip_bfloat16*)d_in[4]; const __hip_bfloat16* woB = (const __hip_bfloat16*)d_in[5];
    char* ws = (char*)d_ws;
    fused_kernel<<<256, 512, 0, stream>>>(xA, wA, xB, wB, ws);
    oproj_kernel<<<224, 512, 0, stream>>>(woA, woB, ws, (__hip_bfloat16*)d_out);
}
```

```cpp
#include <hip/hip_runtime.h>
#include <hip/hip_bf16.h>
#include <stdint.h>

typedef __attribute__((ext_vector_type(8))) short bf16x8;
typedef __attribute__((ext_vector_type(4))) short s16x4;
typedef __attribute__((ext_vector_type(16))) float f32x16;
typedef __attribute__((ext_vector_type(4))) unsigned u32x4;
typedef __attribute__((ext_vector_type(2))) unsigned u32x2;
typedef __attribute__((ext_vector_type(2))) float f32x2;
typedef __attribute__((ext_vector_type(2))) __bf16 b16x2;
typedef __attribute__((address_space(3))) char* lds_ptr;
typedef __attribute__((address_space(3))) const char* lds_cptr;

#define DI __device__ __forceinline__
#define MFMA(a, b, c) __builtin_amdgcn_mfma_f32_32x32x16_bf16((a), (b), (c), 0, 0, 0)

constexpr int SEQ = 2048, DM = 448, NH = 16, DH = 28, N3 = 1344, NTILE = 64;
constexpr size_t QF_OFF = 0;
constexpr size_t KV_OFF = (size_t)4 << 20;
constexpr size_t OF_OFF = (size_t)12 << 20;
constexpr size_t FLAG_OFF = (size_t)16 << 20;
extern "C" __device__ unsigned long long hw_dispatch_id() __asm("llvm.amdgcn.dispatch.id");
constexpr float QSCALE = 0.18898223650461362f * 1.4426950408889634f;

DI unsigned pk2(float a, float b) { f32x2 t = {a, b}; b16x2 r = __builtin_convertvector(t, b16x2); return __builtin_bit_cast(unsigned, r); }
DI bf16x8 pack8(const f32x16& x, int s) {
    u32x4 p = {pk2(x[8 * s], x[8 * s + 1]), pk2(x[8 * s + 2], x[8 * s + 3]), pk2(x[8 * s + 4], x[8 * s + 5]), pk2(x[8 * s + 6], x[8 * s + 7])};
    return __builtin_bit_cast(bf16x8, p);
}
DI void glds16(const void* g, lds_ptr l) {
    const unsigned base = __builtin_amdgcn_readfirstlane((unsigned)(uintptr_t)l); unsigned sv;
    asm volatile("s_mov_b32 %0, m0\n\ts_mov_b32 m0, %2\n\ts_nop 0\n\tglobal_load_lds_dwordx4 %1, off\n\ts_mov_b32 m0, %0" : "=&s"(sv) : "v"(g), "s"(base) : "memory");
}
DI void glds16c(const void* g, lds_ptr l) {
    const unsigned base = __builtin_amdgcn_readfirstlane((unsigned)(uintptr_t)l); unsigned sv;
    asm volatile("s_mov_b32 %0, m0\n\ts_mov_b32 m0, %2\n\ts_nop 0\n\tglobal_load_lds_dwordx4 %1, off sc1\n\ts_mov_b32 m0, %0" : "=&s"(sv) : "v"(g), "s"(base) : "memory");
}
DI bf16x8 ldsr16(lds_cptr p) { return *(const __attribute__((address_space(3))) bf16x8*)p; }
DI s16x4 ldstr(lds_cptr p) { return __builtin_bit_cast(s16x4, __builtin_amdgcn_ds_read_tr16_b64_v4i16((__attribute__((address_space(3))) s16x4*)p)); }
DI bf16x8 cat4(s16x4 a, s16x4 b) { return __builtin_shufflevector(a, b, 0, 1, 2, 3, 4, 5, 6, 7); }
#define STMODE 1
DI void st16(void* p, bf16x8 v) {
    asm volatile("global_store_dwordx4 %0, %1, off sc1\n\ts_nop 1" :: "v"(p), "v"(v) : "memory");
}
#define WAITV(n) asm volatile("s_waitcnt vmcnt(" #n ")" ::: "memory")
#define BAR() do { asm volatile("s_waitcnt lgkmcnt(0)" ::: "memory"); __builtin_amdgcn_s_barrier(); asm volatile("" ::: "memory"); } while (0)
DI float swap_halves(float v) {
    auto r = __builtin_amdgcn_permlane32_swap(__float_as_uint(v), __float_as_uint(v), false, false);
    return (threadIdx.x & 32) ? __uint_as_float(r[0]) : __uint_as_float(r[1]);
}

constexpr int F_XB = 32768, F_WB = 12288, F_STG = F_XB + F_WB;
constexpr int A_CH = 16384, A_GRP = 4 * A_CH, F_FLAGS = 3 * F_STG, F_MX = F_FLAGS + 256, F_LDS = F_MX + 2048;
DI float rowmax16(const f32x16& s) {
    float t = fmaxf(s[0], s[1]);
#pragma unroll
    for (int r = 2; r < 16; r += 2) t = fmaxf(fmaxf(t, s[r]), s[r + 1]);
    return fmaxf(t, swap_halves(t));
}
#define A_STAGE(c) do { const char* s_ = kvh + (size_t)((((qblk + (c)) & 7) * 8) * 4096); _Pragma("unroll") for (int i_ = 0; i_ < 4; ++i_) glds16c(s_ + i_ * 1024, dst + ((c) & 3) * A_CH + i_ * 1024); } while (0)
#define EXP16(s) do { _Pragma("unroll") for (int r_ = 0; r_ < 16; ++r_) s[r_] = __builtin_amdgcn_exp2f(s[r_]); } while (0)

DI void attn_safe(const char* __restrict__ kvh, int qblk, lds_ptr dst, lds_cptr rd, bf16x8 qa0, bf16x8 qa1, bf16x8 qb0, bf16x8 qb1, f32x16& oA, f32x16& oB, float& mA, float& mB) {
    A_STAGE(0); A_STAGE(1); A_STAGE(2);
    oA = f32x16{}; oB = f32x16{}; mA = -INFINITY; mB = -INFINITY;
    for (int c = 0; c < 8; ++c) {
        if (c < 6) WAITV(8); else if (c == 6) WAITV(4); else WAITV(0);
        BAR();
        if (c + 3 < 8) A_STAGE(c + 3);
        const lds_cptr cb = rd + (c & 3) * A_CH;
        for (int t = 0; t < 4; ++t) {
            const bf16x8 k0 = ldsr16(cb + t * 4096), k1 = ldsr16(cb + t * 4096 + 1024), v0 = ldsr16(cb + t * 4096 + 2048), v1 = ldsr16(cb + t * 4096 + 3072);
            const f32x16 z = {};
            f32x16 s = MFMA(k0, qa0, z); s = MFMA(k1, qa1, s);
            float mn = fmaxf(mA, rowmax16(s)), al = __builtin_amdgcn_exp2f(mA - mn); mA = mn;
#pragma unroll
            for (int r = 0; r < 16; ++r) { oA[r] *= al; s[r] = __builtin_amdgcn_exp2f(s[r] - mn); }
            oA = MFMA(v0, pack8(s, 0), oA); oA = MFMA(v1, pack8(s, 1), oA);
            s = MFMA(k0, qb0, z); s = MFMA(k1, qb1, s);
            mn = fmaxf(mB, rowmax16(s)); al = __builtin_amdgcn_exp2f(mB - mn); mB = mn;
#pragma unroll
            for (int r = 0; r < 16; ++r) { oB[r] *= al; s[r] = __builtin_amdgcn_exp2f(s[r] - mn); }
            oB = MFMA(v0, pack8(s, 0), oB); oB = MFMA(v1, pack8(s, 1), oB);
        }
    }
}

__global__ __launch_bounds__(512) void fused_kernel(const __hip_bfloat16* __restrict__ xA, const __hip_bfloat16* __restrict__ wA, const __hip_bfloat16* __restrict__ woA,
                                                    const __hip_bfloat16* __restrict__ xB, const __hip_bfloat16* __restrict__ wB, const __hip_bfloat16* __restrict__ woB,
                                                    char* __restrict__ ws, __hip_bfloat16* __restrict__ out) {
    __shared__ __attribute__((aligned(16))) char smem[F_LDS];
    const lds_ptr lds = (lds_ptr)smem;
    const int tid = threadIdx.x, lane = tid & 63, w = __builtin_amdgcn_readfirstlane(tid >> 6);
    const int h = lane >> 5, r32 = lane & 31;
    const int bid = blockIdx.x, xcd = bid & 7, idx = bid >> 3, slot = xcd + 8 * (idx >> 3), qblk = idx & 7;
    const int head = slot & 15;
    {
        const __hip_bfloat16* x = (slot >> 4) ? xB : xA; const __hip_bfloat16* W = (slot >> 4) ? wB : wA;
        const __hip_bfloat16* xsrc[4]; const __hip_bfloat16* wsrc[2];
#pragma unroll
        for (int i = 0; i < 4; ++i) { const int row = 32 * w + 8 * i + (lane >> 3), c = (lane & 7) ^ ((row >> 1) & 7); xsrc[i] = x + (size_t)(qblk * 256 + row) * DM + 8 * c; }
        const int j0 = (w < 4) ? 2 * w : 4 + w;
#pragma unroll
        for (int i = 0; i < 2; ++i) { const int j = j0 + i, row = 16 * (j & 3) + (lane >> 2), pc = lane & 3;
            wsrc[i] = W + (size_t)row * N3 + (j >> 2) * DM + head * DH + (pc == 3 ? 20 : 8 * pc); }
#define F_STAGE(kc, sl) do { _Pragma("unroll") for (int i = 0; i < 4; ++i) glds16(xsrc[i] + (kc) * 64, lds + (sl) * F_STG + (4 * w + i) * 1024); \
        glds16(wsrc[0] + (size_t)(kc) * 64 * N3, lds + (sl) * F_STG + F_XB + j0 * 1024); \
        if (w < 4) glds16(wsrc[1] + (size_t)(kc) * 64 * N3, lds + (sl) * F_STG + F_XB + (j0 + 1) * 1024); } while (0)
        F_STAGE(0, 0); F_STAGE(1, 1);
        const int xrow = 32 * w + r32, xsw = (xrow >> 1) & 7, col4 = ((lane >> 4) & 1) * 4 + (lane & 3);
        const int wbase = F_XB + (8 * h + ((lane & 15) >> 2)) * 64 + (col4 <= 5 ? col4 * 8 : 56);
        f32x16 aq = {}, ak = {}, av = {};
#pragma unroll
        for (int kc = 0; kc < 7; ++kc) {
            if (kc < 6) { if (w < 4) WAITV(6); else WAITV(5); } else WAITV(0);
            BAR();
            if (kc + 2 < 7) F_STAGE(kc + 2, (kc + 2) % 3);
            const lds_cptr xs = lds + (kc % 3) * F_STG, wsb = xs + wbase;
#pragma unroll
            for (int ks = 0; ks < 4; ++ks) {
                const bf16x8 xf = ldsr16(xs + xrow * 128 + (((2 * ks + h) ^ xsw) * 16));
                const bf16x8 wq = cat4(ldstr(wsb + ks * 1024), ldstr(wsb + ks * 1024 + 256));
                const bf16x8 wk = cat4(ldstr(wsb + ks * 1024 + 4096), ldstr(wsb + ks * 1024 + 4096 + 256));
                const bf16x8 wv = cat4(ldstr(wsb + ks * 1024 + 8192), ldstr(wsb + ks * 1024 + 8192 + 256));
                aq = MFMA(wq, xf, aq); ak = MFMA(wk, xf, ak); av = MFMA(xf, wv, av);
            }
        }
#undef F_STAGE
#pragma unroll
        for (int r = 0; r < 16; ++r) aq[r] *= QSCALE;
        if (h) {
#pragma unroll
            for (int r = 12; r < 16; ++r) { aq[r] = 0.f; ak[r] = 0.f; }
            ak[12] = 1.0f;
        }
        if (r32 >= 28) { const float f = (r32 == 28) ? 1.f : 0.f;
#pragma unroll
            for (int r = 0; r < 16; ++r) av[r] = f; }
        const bf16x8 fk0 = pack8(ak, 0), fk1 = pack8(ak, 1), fv0 = pack8(av, 0), fv1 = pack8(av, 1);
        char* kd = ws + KV_OFF + ((size_t)(slot * 64 + qblk * 8 + w)) * 4096 + lane * 16;
        st16(kd, fk0); st16(kd + 1024, fk1); st16(kd + 2048, fv0); st16(kd + 3072, fv1);
        BAR();
        typedef __attribute__((address_space(3))) bf16x8* lds_frag;
        lds_frag kl = (lds_frag)(lds + (w >> 2) * A_GRP + (w & 3) * 4096 + lane * 16);
        kl[0] = fk0; kl[64] = fk1; kl[128] = fv0; kl[192] = fv1;
        lds_frag ql = (lds_frag)(lds + 3 * A_CH + w * 2048 + lane * 16);
        ql[0] = pack8(aq, 0); ql[64] = pack8(aq, 1);
        BAR();
    }
    const int qg = w & 3, kh = w >> 2, T0 = qblk * 8 + 2 * qg;
    const char* kvh = ws + KV_OFF + ((size_t)(slot * 64 + kh * 4 + qg)) * 4096 + lane * 16;
    const lds_ptr dst = lds + kh * A_GRP + qg * 4096;
    const lds_cptr rd = lds + kh * A_GRP + lane * 16;
    const bf16x8 qA0 = ldsr16(lds + 3 * A_CH + qg * 4096 + lane * 16), qA1 = ldsr16(lds + 3 * A_CH + qg * 4096 + 1024 + lane * 16);
    const bf16x8 qB0 = ldsr16(lds + 3 * A_CH + qg * 4096 + 2048 + lane * 16), qB1 = ldsr16(lds + 3 * A_CH + qg * 4096 + 3072 + lane * 16);
    const f32x16 z = {};
    f32x16 oA = {}, oB = {}, sA, sB, sAn, sBn;
    float mA, mB;
    bf16x8 k0 = ldsr16(rd), k1 = ldsr16(rd + 1024), v0, v1, v0n, v1n;
    {
        sA = MFMA(k0, qA0, z); sA = MFMA(k1, qA1, sA); sB = MFMA(k0, qB0, z); sB = MFMA(k1, qB1, sB);
        const unsigned pm = pk2(-rowmax16(sA), -rowmax16(sB));
        mA = -__uint_as_float(pm << 16); mB = -__uint_as_float(pm & 0xffff0000u);
#pragma unroll
        for (int r = 0; r < 16; ++r) { sA[r] -= mA; sB[r] -= mB; }
    }
    bf16x8 qA1p = qA1, qB1p = qB1;
    if (h) { qA1p[4] = (short)(__float_as_uint(-mA) >> 16); qB1p[4] = (short)(__float_as_uint(-mB) >> 16); }
    const unsigned long long token = (hw_dispatch_id() * 0x9E3779B97F4A7C15ull) ^ ((unsigned long long)(uintptr_t)__builtin_amdgcn_dispatch_ptr() << 1) ^ 0x5851F42D4C957F2Dull;
    unsigned long long* flags_g = (unsigned long long*)(ws + FLAG_OFF) + slot * 8;
    const bool act3 = head < 14; const int tq = head / 7, nb = head % 7;
    const __hip_bfloat16* wo3 = ((slot >> 4) ? woB : woA) + nb * 64 + (((lane & 7) >> 2) ^ ((lane >> 4) & 1)) * 32 + (lane & 3) * 8 + (size_t)(lane >> 3) * DM;
#define WO_INS(ii) glds16(wo3 + (size_t)(ii) * 8 * DM, lds + (ii) * 1024 + ((ii) >= 32 ? 32768 : 0))
#define WO_STAGE1() do { _Pragma("unroll") for (int i_ = 0; i_ < 2; ++i_) { WO_INS(2 * w + i_); WO_INS(32 + 2 * w + i_); } } while (0)
#define WO_STAGE2() do { _Pragma("unroll") for (int i_ = 0; i_ < 2; ++i_) WO_INS(16 + 2 * w + i_); WO_INS(48 + w); } while (0)
#pragma unroll
    for (int t = 0; t < 4; ++t) {
        if (t == 2) {
            WAITV(0); BAR();
            if (tid == 0) __hip_atomic_store(flags_g + qblk, token, __ATOMIC_RELAXED, __HIP_MEMORY_SCOPE_AGENT);
        }
        if (t == 3) {
            for (unsigned spins = 0; spins < (1u << 22); ++spins) {
                const unsigned long long f = __hip_atomic_load(flags_g + (lane & 7), __ATOMIC_RELAXED, __HIP_MEMORY_SCOPE_AGENT);
                if (__all(f == token)) break;
                __builtin_amdgcn_s_sleep(2);
            }
            A_STAGE(1); A_STAGE(2); A_STAGE(3);
        }
        if (t > 0) { k0 = ldsr16(rd + t * 4096); k1 = ldsr16(rd + t * 4096 + 1024);
            sA = MFMA(k0, qA0, z); sA = MFMA(k1, qA1p, sA); sB = MFMA(k0, qB0, z); sB = MFMA(k1, qB1p, sB); }
        v0 = ldsr16(rd + t * 4096 + 2048); v1 = ldsr16(rd + t * 4096 + 3072);
        EXP16(sA);
        oA = MFMA(v0, pack8(sA, 0), oA); oA = MFMA(v1, pack8(sA, 1), oA);
        EXP16(sB);
        oB = MFMA(v0, pack8(sB, 0), oB); oB = MFMA(v1, pack8(sB, 1), oB);
    }
    WAITV(8); BAR();
    k0 = ldsr16(rd + A_CH); k1 = ldsr16(rd + A_CH + 1024);
    sA = MFMA(k0, qA0, z); sA = MFMA(k1, qA1p, sA); sB = MFMA(k0, qB0, z); sB = MFMA(k1, qB1p, sB);
    v0 = ldsr16(rd + A_CH + 2048); v1 = ldsr16(rd + A_CH + 3072);
    k0 = ldsr16(rd + A_CH + 4096); k1 = ldsr16(rd + A_CH + 4096 + 1024);
    for (int c = 1; c < 8; ++c) {
        const lds_cptr cb = rd + (c & 3) * A_CH, cn = rd + ((c + 1) & 3) * A_CH;
#pragma unroll
        for (int t = 0; t < 4; ++t) {
            const bool last = (c == 7 && t == 3);
            if (t == 2 && c < 7) {
                if (c < 6) WAITV(4); else WAITV(0);
                BAR();
                if (c + 3 < 8) A_STAGE(c + 3);
                if (act3) { if (c == 5) WO_STAGE1(); if (c == 6) WO_STAGE2(); }
            }
            if (!last) { sAn = MFMA(k0, qA0, z); sAn = MFMA(k1, qA1p, sAn); sBn = MFMA(k0, qB0, z); sBn = MFMA(k1, qB1p, sBn); }
            if (!last) { const lds_cptr vp = (t < 3) ? cb + (t + 1) * 4096 : cn; v0n = ldsr16(vp + 2048); v1n = ldsr16(vp + 3072); }
            if (!(c == 7 && t >= 2)) { const lds_cptr kp = (t < 2) ? cb + (t + 2) * 4096 : cn + (t - 2) * 4096; k0 = ldsr16(kp); k1 = ldsr16(kp + 1024); }
            EXP16(sA);
            oA = MFMA(v0, pack8(sA, 0), oA); oA = MFMA(v1, pack8(sA, 1), oA);
            EXP16(sB);
            oB = MFMA(v0, pack8(sB, 0), oB); oB = MFMA(v1, pack8(sB, 1), oB);
            sA = sAn; sB = sBn; v0 = v0n; v1 = v1n;
        }
    }
    __attribute__((address_space(3))) int* flags = (__attribute__((address_space(3))) int*)(lds + F_FLAGS);
    const bool bad = h && !(fmaxf(oA[12], oB[12]) < 1.0e24f && oA[12] == oA[12] && oB[12] == oB[12]);
    const int anybad = __any(bad);
    if (lane == 0) flags[w] = anybad;
    BAR();
    typedef __attribute__((ext_vector_type(4))) float f32x4;
    __attribute__((address_space(3))) f32x4* xw = (__attribute__((address_space(3))) f32x4*)(lds + 32768 + (qg * 2 + kh) * 4096);
    const __attribute__((address_space(3))) f32x4* xr = (const __attribute__((address_space(3))) f32x4*)(lds + 32768 + (qg * 2 + (kh ^ 1)) * 4096);
    __attribute__((address_space(3))) float* mxw = (__attribute__((address_space(3))) float*)(lds + F_MX) + (qg * 2 + kh) * 64;
    const __attribute__((address_space(3))) float* mxr = (const __attribute__((address_space(3))) float*)(lds + F_MX) + (qg * 2 + (kh ^ 1)) * 64;
#define A_PUT() do { const f32x16& og = kh ? oA : oB; _Pragma("unroll") for (int g = 0; g < 4; ++g) xw[g * 64 + lane] = f32x4{og[4 * g], og[4 * g + 1], og[4 * g + 2], og[4 * g + 3]}; \
        mxw[lane] = kh ? mA : mB; } while (0)
    A_PUT();
    const int redo = __any(flags[lane & 7]);
    BAR();
    if (redo) {
        BAR();
        WAITV(0);
        attn_safe(kvh, qblk, dst, rd, qA0, qA1, qB0, qB1, oA, oB, mA, mB);
        BAR();
        A_PUT();
        if (act3) { WO_STAGE1(); WO_STAGE2(); }
        BAR();
    }
    {
        f32x16 o = kh ? oB : oA; const float m = kh ? mB : mA;
        const float m2 = mxr[lane], mn = fmaxf(m, m2), a1 = __builtin_amdgcn_exp2f(m - mn), a2 = __builtin_amdgcn_exp2f(m2 - mn);
#pragma unroll
        for (int g = 0; g < 4; ++g) { const f32x4 t = xr[g * 64 + lane];
#pragma unroll
            for (int j = 0; j < 4; ++j) o[4 * g + j] = a1 * o[4 * g + j] + a2 * t[j]; }
        const float lo = swap_halves(o[12]);
        const float l = h ? o[12] : lo, inv = 1.0f / l;
#pragma unroll
        for (int r = 0; r < 16; ++r) o[r] *= inv;
        if (h) {
#pragma unroll
            for (int r = 12; r < 16; ++r) o[r] = 0.f;
        }
        char* od = ws + OF_OFF + ((size_t)(((slot >> 4) * 64 + T0 + kh) * 16 + (slot & 15))) * 2048 + lane * 16;
        st16(od, pack8(o, 0)); st16(od + 1024, pack8(o, 1));
    }
    unsigned long long* oflags = (unsigned long long*)(ws + FLAG_OFF + 4096) + ((slot >> 4) * 8 + qblk) * 16;
    WAITV(0); BAR();
    if (tid == 0) __hip_atomic_store(oflags + head, token, __ATOMIC_RELAXED, __HIP_MEMORY_SCOPE_AGENT);
    if (!act3) return;
    for (unsigned spins = 0; spins < (1u << 16); ++spins) {
        const unsigned long long f = __hip_atomic_load(oflags + (lane & 15), __ATOMIC_RELAXED, __HIP_MEMORY_SCOPE_AGENT);
        if (__all(f == token)) break;
        __builtin_amdgcn_s_sleep(2);
    }
    asm volatile("" ::: "memory");
    {
        const int tl = w & 3, nt = w >> 2, T = qblk * 8 + 4 * tq + tl;
        const auto rs = __builtin_amdgcn_make_buffer_rsrc((void*)(ws + OF_OFF), 0, 4 << 20, 0x00020000);
        const int obase = (((slot >> 4) * 64 + T) * 16) * 2048 + lane * 16;
        bf16x8 of[32];
#pragma unroll
        for (int i = 0; i < 32; ++i) of[i] = __builtin_bit_cast(bf16x8, __builtin_amdgcn_raw_buffer_load_b128(rs, obase + i * 1024, 0, 16));
        const int q = (lane & 15) >> 2, cb = ((lane >> 4) & 1) * 32 + (lane & 3) * 8;
        f32x16 acc = {}, acc2 = {};
#pragma unroll
        for (int hd = 0; hd < 16; ++hd) {
#pragma unroll
            for (int s = 0; s < 2; ++s) {
                s16x4 f[2];
#pragma unroll
                for (int r = 0; r < 2; ++r) { int k = hd * 28 + 16 * s + 8 * r + 4 * h + q; if (hd == 15 && s == 1 && r == 1) k = min(k, 447);
                    f[r] = ldstr(lds + k * 128 + ((k >> 8) << 15) + ((nt ^ ((k >> 1) & 1)) * 64) + cb); }
                if (s) acc2 = MFMA(cat4(f[0], f[1]), of[hd * 2 + s], acc2); else acc = MFMA(cat4(f[0], f[1]), of[hd * 2 + s], acc);
            }
        }
        __hip_bfloat16* od = out + (size_t)(T * 32 + r32) * 896 + (slot >> 4) * 448 + nb * 64 + nt * 32 + 4 * h;
#pragma unroll
        for (int g = 0; g < 4; ++g) { u32x2 v = {pk2(acc[4 * g] + acc2[4 * g], acc[4 * g + 1] + acc2[4 * g + 1]), pk2(acc[4 * g + 2] + acc2[4 * g + 2], acc[4 * g + 3] + acc2[4 * g + 3])}; *(u32x2*)(od + 8 * g) = v; }
    }
}

extern "C" void kernel_launch(void* const* d_in, const int* in_sizes, int n_in, void* d_out, int out_size, void* d_ws, size_t ws_size, hipStream_t stream) {
    const __hip_bfloat16* xA = (const __hip_bfloat16*)d_in[0]; const __hip_bfloat16* wA = (const __hip_bfloat16*)d_in[1]; const __hip_bfloat16* woA = (const __hip_bfloat16*)d_in[2];
    const __hip_bfloat16* xB = (const __hip_bfloat16*)d_in[3]; const __hip_bfloat16* wB = (const __hip_bfloat16*)d_in[4]; const __hip_bfloat16* woB = (const __hip_bfloat16*)d_in[5];
    char* ws = (char*)d_ws;
    fused_kernel<<<256, 512, 0, stream>>>(xA, wA, woA, xB, wB, woB, ws, (__hip_bfloat16*)d_out);
}
```

```cpp
#include <hip/hip_runtime.h>
#include <hip/hip_bf16.h>
#include <stdint.h>

typedef __attribute__((ext_vector_type(8))) short bf16x8;
typedef __attribute__((ext_vector_type(4))) short s16x4;
typedef __attribute__((ext_vector_type(16))) float f32x16;
typedef __attribute__((ext_vector_type(4))) unsigned u32x4;
typedef __attribute__((ext_vector_type(2))) unsigned u32x2;
typedef __attribute__((ext_vector_type(2))) float f32x2;
typedef __attribute__((ext_vector_type(2))) __bf16 b16x2;
typedef __attribute__((address_space(3))) char* lds_ptr;
typedef __attribute__((address_space(3))) const char* lds_cptr;

#define DI __device__ __forceinline__
#define MFMA(a, b, c) __builtin_amdgcn_mfma_f32_32x32x16_bf16((a), (b), (c), 0, 0, 0)

constexpr int SEQ = 2048, DM = 448, NH = 16, DH = 28, N3 = 1344, NTILE = 64;
constexpr size_t QF_OFF = 0;
constexpr size_t KV_OFF = (size_t)4 << 20;
constexpr size_t OF_OFF = (size_t)12 << 20;
constexpr size_t FLAG_OFF = (size_t)16 << 20;
extern "C" __device__ unsigned long long hw_dispatch_id() __asm("llvm.amdgcn.dispatch.id");
constexpr float QSCALE = 0.18898223650461362f * 1.4426950408889634f;

DI unsigned pk2(float a, float b) { f32x2 t = {a, b}; b16x2 r = __builtin_convertvector(t, b16x2); return __builtin_bit_cast(unsigned, r); }
DI bf16x8 pack8(const f32x16& x, int s) {
    u32x4 p = {pk2(x[8 * s], x[8 * s + 1]), pk2(x[8 * s + 2], x[8 * s + 3]), pk2(x[8 * s + 4], x[8 * s + 5]), pk2(x[8 * s + 6], x[8 * s + 7])};
    return __builtin_bit_cast(bf16x8, p);
}
DI void glds16(const void* g, lds_ptr l) {
    const unsigned base = __builtin_amdgcn_readfirstlane((unsigned)(uintptr_t)l); unsigned sv;
    asm volatile("s_mov_b32 %0, m0\n\ts_mov_b32 m0, %2\n\ts_nop 0\n\tglobal_load_lds_dwordx4 %1, off\n\ts_mov_b32 m0, %0" : "=&s"(sv) : "v"(g), "s"(base) : "memory");
}
DI void glds16c(const void* g, lds_ptr l) {
    const unsigned base = __builtin_amdgcn_readfirstlane((unsigned)(uintptr_t)l); unsigned sv;
    asm volatile("s_mov_b32 %0, m0\n\ts_mov_b32 m0, %2\n\ts_nop 0\n\tglobal_load_lds_dwordx4 %1, off sc1\n\ts_mov_b32 m0, %0" : "=&s"(sv) : "v"(g), "s"(base) : "memory");
}
DI bf16x8 ldsr16(lds_cptr p) { return *(const __attribute__((address_space(3))) bf16x8*)p; }
DI s16x4 ldstr(lds_cptr p) { return __builtin_bit_cast(s16x4, __builtin_amdgcn_ds_read_tr16_b64_v4i16((__attribute__((address_space(3))) s16x4*)p)); }
DI bf16x8 cat4(s16x4 a, s16x4 b) { return __builtin_shufflevector(a, b, 0, 1, 2, 3, 4, 5, 6, 7); }
#define STMODE 1
DI void st16(void* p, bf16x8 v) {
    asm volatile("global_store_dwordx4 %0, %1, off sc1\n\ts_nop 1" :: "v"(p), "v"(v) : "memory");
}
#define WAITV(n) asm volatile("s_waitcnt vmcnt(" #n ")" ::: "memory")
#define BAR() do { asm volatile("s_waitcnt lgkmcnt(0)" ::: "memory"); __builtin_amdgcn_s_barrier(); asm volatile("" ::: "memory"); } while (0)
DI float swap_halves(float v) {
    auto r = __builtin_amdgcn_permlane32_swap(__float_as_uint(v), __float_as_uint(v), false, false);
    return (threadIdx.x & 32) ? __uint_as_float(r[0]) : __uint_as_float(r[1]);
}

constexpr int F_XB = 32768, F_WB = 12288, F_STG = F_XB + F_WB;
constexpr int A_CH = 16384, A_GRP = 4 * A_CH, F_FLAGS = 3 * F_STG, F_MX = F_FLAGS + 256, F_LDS = F_MX + 2048;
DI float rowmax16(const f32x16& s) {
    float t = fmaxf(s[0], s[1]);
#pragma unroll
    for (int r = 2; r < 16; r += 2) t = fmaxf(fmaxf(t, s[r]), s[r + 1]);
    return fmaxf(t, swap_halves(t));
}
#define A_STAGE(c) do { const char* s_ = kvh + (size_t)((((qblk + (c)) & 7) * 8) * 4096); _Pragma("unroll") for (int i_ = 0; i_ < 4; ++i_) glds16c(s_ + i_ * 1024, dst + ((c) & 3) * A_CH + i_ * 1024); } while (0)
#define EXP16(s) do { _Pragma("unroll") for (int r_ = 0; r_ < 16; ++r_) s[r_] = __builtin_amdgcn_exp2f(s[r_]); } while (0)

DI void attn_safe(const char* __restrict__ kvh, int qblk, lds_ptr dst, lds_cptr rd, bf16x8 qa0, bf16x8 qa1, bf16x8 qb0, bf16x8 qb1, f32x16& oA, f32x16& oB, float& mA, float& mB) {
    A_STAGE(0); A_STAGE(1); A_STAGE(2);
    oA = f32x16{}; oB = f32x16{}; mA = -INFINITY; mB = -INFINITY;
    for (int c = 0; c < 8; ++c) {
        if (c < 6) WAITV(8); else if (c == 6) WAITV(4); else WAITV(0);
        BAR();
        if (c + 3 < 8) A_STAGE(c + 3);
        const lds_cptr cb = rd + (c & 3) * A_CH;
        for (int t = 0; t < 4; ++t) {
            const bf16x8 k0 = ldsr16(cb + t * 4096), k1 = ldsr16(cb + t * 4096 + 1024), v0 = ldsr16(cb + t * 4096 + 2048), v1 = ldsr16(cb + t * 4096 + 3072);
            const f32x16 z = {};
            f32x16 s = MFMA(k0, qa0, z); s = MFMA(k1, qa1, s);
            float mn = fmaxf(mA, rowmax16(s)), al = __builtin_amdgcn_exp2f(mA - mn); mA = mn;
#pragma unroll
            for (int r = 0; r < 16; ++r) { oA[r] *= al; s[r] = __builtin_amdgcn_exp2f(s[r] - mn); }
            oA = MFMA(v0, pack8(s, 0), oA); oA = MFMA(v1, pack8(s, 1), oA);
            s = MFMA(k0, qb0, z); s = MFMA(k1, qb1, s);
            mn = fmaxf(mB, rowmax16(s)); al = __builtin_amdgcn_exp2f(mB - mn); mB = mn;
#pragma unroll
            for (int r = 0; r < 16; ++r) { oB[r] *= al; s[r] = __builtin_amdgcn_exp2f(s[r] - mn); }
            oB = MFMA(v0, pack8(s, 0), oB); oB = MFMA(v1, pack8(s, 1), oB);
        }
    }
}

__global__ __launch_bounds__(512) void fused_kernel(const __hip_bfloat16* __restrict__ xA, const __hip_bfloat16* __restrict__ wA, const __hip_bfloat16* __restrict__ woA,
                                                    const __hip_bfloat16* __restrict__ xB, const __hip_bfloat16* __restrict__ wB, const __hip_bfloat16* __restrict__ woB,
                                                    char* __restrict__ ws, __hip_bfloat16* __restrict__ out) {
    __shared__ __attribute__((aligned(16))) char smem[F_LDS];
    const lds_ptr lds = (lds_ptr)smem;
    const int tid = threadIdx.x, lane = tid & 63, w = __builtin_amdgcn_readfirstlane(tid >> 6);
    const int h = lane >> 5, r32 = lane & 31;
    const int bid = blockIdx.x, xcd = bid & 7, idx = bid >> 3, slot = xcd + 8 * (idx >> 3), qblk = idx & 7;
    const int head = slot & 15;
    {
        const __hip_bfloat16* x = (slot >> 4) ? xB : xA; const __hip_bfloat16* W = (slot >> 4) ? wB : wA;
        const __hip_bfloat16* xsrc[4]; const __hip_bfloat16* wsrc[2];
#pragma unroll
        for (int i = 0; i < 4; ++i) { const int row = 32 * w + 8 * i + (lane >> 3), c = (lane & 7) ^ ((row >> 1) & 7); xsrc[i] = x + (size_t)(qblk * 256 + row) * DM + 8 * c; }
        const int j0 = (w < 4) ? 2 * w : 4 + w;
#pragma unroll
        for (int i = 0; i < 2; ++i) { const int j = j0 + i, row = 16 * (j & 3) + (lane >> 2), pc = lane & 3;
            wsrc[i] = W + (size_t)row * N3 + (j >> 2) * DM + head * DH + (pc == 3 ? 20 : 8 * pc); }
#define F_STAGE(kc, sl) do { _Pragma("unroll") for (int i = 0; i < 4; ++i) glds16(xsrc[i] + (kc) * 64, lds + (sl) * F_STG + (4 * w + i) * 1024); \
        glds16(wsrc[0] + (size_t)(kc) * 64 * N3, lds + (sl) * F_STG + F_XB + j0 * 1024); \
        if (w < 4) glds16(wsrc[1] + (size_t)(kc) * 64 * N3, lds + (sl) * F_STG + F_XB + (j0 + 1) * 1024); } while (0)
        F_STAGE(0, 0); F_STAGE(1, 1);
        const int xrow = 32 * w + r32, xsw = (xrow >> 1) & 7, col4 = ((lane >> 4) & 1) * 4 + (lane & 3);
        const int wbase = F_XB + (8 * h + ((lane & 15) >> 2)) * 64 + (col4 <= 5 ? col4 * 8 : 56);
        f32x16 aq = {}, ak = {}, av = {};
#pragma unroll
        for (int kc = 0; kc < 7; ++kc) {
            if (kc < 6) { if (w < 4) WAITV(6); else WAITV(5); } else WAITV(0);
            BAR();
            if (kc + 2 < 7) F_STAGE(kc + 2, (kc + 2) % 3);
            const lds_cptr xs = lds + (kc % 3) * F_STG, wsb = xs + wbase;
#pragma unroll
            for (int ks = 0; ks < 4; ++ks) {
                const bf16x8 xf = ldsr16(xs + xrow * 128 + (((2 * ks + h) ^ xsw) * 16));
                const bf16x8 wq = cat4(ldstr(wsb + ks * 1024), ldstr(wsb + ks * 1024 + 256));
                const bf16x8 wk = cat4(ldstr(wsb + ks * 1024 + 4096), ldstr(wsb + ks * 1024 + 4096 + 256));
                const bf16x8 wv = cat4(ldstr(wsb + ks * 1024 + 8192), ldstr(wsb + ks * 1024 + 8192 + 256));
                aq = MFMA(wq, xf, aq); ak = MFMA(wk, xf, ak); av = MFMA(xf, wv, av);
            }
        }
#undef F_STAGE
#pragma unroll
        for (int r = 0; r < 16; ++r) aq[r] *= QSCALE;
        if (h) {
#pragma unroll
            for (int r = 12; r < 16; ++r) { aq[r] = 0.f; ak[r] = 0.f; }
            ak[12] = 1.0f;
        }
        if (r32 >= 28) { const float f = (r32 == 28) ? 1.f : 0.f;
#pragma unroll
            for (int r = 0; r < 16; ++r) av[r] = f; }
        const bf16x8 fk0 = pack8(ak, 0), fk1 = pack8(ak, 1), fv0 = pack8(av, 0), fv1 = pack8(av, 1);
        char* kd = ws + KV_OFF + ((size_t)(slot * 64 + qblk * 8 + w)) * 4096 + lane * 16;
        st16(kd, fk0); st16(kd + 1024, fk1); st16(kd + 2048, fv0); st16(kd + 3072, fv1);
        BAR();
        typedef __attribute__((address_space(3))) bf16x8* lds_frag;
        lds_frag kl = (lds_frag)(lds + (w >> 2) * A_GRP + (w & 3) * 4096 + lane * 16);
        kl[0] = fk0; kl[64] = fk1; kl[128] = fv0; kl[192] = fv1;
        lds_frag ql = (lds_frag)(lds + 3 * A_CH + w * 2048 + lane * 16);
        ql[0] = pack8(aq, 0); ql[64] = pack8(aq, 1);
        BAR();
    }
    const int qg = w & 3, kh = w >> 2, T0 = qblk * 8 + 2 * qg;
    const char* kvh = ws + KV_OFF + ((size_t)(slot * 64 + kh * 4 + qg)) * 4096 + lane * 16;
    const lds_ptr dst = lds + kh * A_GRP + qg * 4096;
    const lds_cptr rd = lds + kh * A_GRP + lane * 16;
    const bf16x8 qA0 = ldsr16(lds + 3 * A_CH + qg * 4096 + lane * 16), qA1 = ldsr16(lds + 3 * A_CH + qg * 4096 + 1024 + lane * 16);
    const bf16x8 qB0 = ldsr16(lds + 3 * A_CH + qg * 4096 + 2048 + lane * 16), qB1 = ldsr16(lds + 3 * A_CH + qg * 4096 + 3072 + lane * 16);
    const f32x16 z = {};
    f32x16 oA = {}, oB = {}, sA, sB, sAn, sBn;
    float mA, mB;
    bf16x8 k0 = ldsr16(rd), k1 = ldsr16(rd + 1024), v0, v1, v0n, v1n;
    {
        sA = MFMA(k0, qA0, z); sA = MFMA(k1, qA1, sA); sB = MFMA(k0, qB0, z); sB = MFMA(k1, qB1, sB);
        const unsigned pm = pk2(-rowmax16(sA), -rowmax16(sB));
        mA = -__uint_as_float(pm << 16); mB = -__uint_as_float(pm & 0xffff0000u);
#pragma unroll
        for (int r = 0; r < 16; ++r) { sA[r] -= mA; sB[r] -= mB; }
    }
    bf16x8 qA1p = qA1, qB1p = qB1;
    if (h) { qA1p[4] = (short)(__float_as_uint(-mA) >> 16); qB1p[4] = (short)(__float_as_uint(-mB) >> 16); }
    const unsigned long long token = (hw_dispatch_id() * 0x9E3779B97F4A7C15ull) ^ ((unsigned long long)(uintptr_t)__builtin_amdgcn_dispatch_ptr() << 1) ^ 0x5851F42D4C957F2Dull;
    unsigned long long* flags_g = (unsigned long long*)(ws + FLAG_OFF) + slot * 8;
    const bool act3 = idx < 28; const int quad = 4 * xcd + idx / 7, nb = idx % 7, p3 = quad >> 4;
    const __hip_bfloat16* wo3 = (p3 ? woB : woA) + nb * 64 + (((lane & 7) >> 2) ^ ((lane >> 4) & 1)) * 32 + (lane & 3) * 8 + (size_t)(lane >> 3) * DM;
#define WO_INS(ii) glds16(wo3 + (size_t)(ii) * 8 * DM, lds + (ii) * 1024 + ((ii) >= 32 ? 32768 : 0))
#define WO_STAGE1() do { _Pragma("unroll") for (int i_ = 0; i_ < 2; ++i_) { WO_INS(2 * w + i_); WO_INS(32 + 2 * w + i_); } } while (0)
#define WO_STAGE2() do { _Pragma("unroll") for (int i_ = 0; i_ < 2; ++i_) WO_INS(16 + 2 * w + i_); WO_INS(48 + w); } while (0)
#pragma unroll
    for (int t = 0; t < 4; ++t) {
        if (t == 1) {
            WAITV(0); BAR();
            if (tid == 0) __hip_atomic_store(flags_g + qblk, token, __ATOMIC_RELAXED, __HIP_MEMORY_SCOPE_AGENT);
        }
        if (t == 2) {
            for (unsigned spins = 0; spins < (1u << 22); ++spins) {
                const unsigned long long f = __hip_atomic_load(flags_g + (lane & 7), __ATOMIC_RELAXED, __HIP_MEMORY_SCOPE_AGENT);
                if (__all(f == token)) break;
                __builtin_amdgcn_s_sleep(2);
            }
            A_STAGE(1); A_STAGE(2); A_STAGE(3);
        }
        if (t > 0) { k0 = ldsr16(rd + t * 4096); k1 = ldsr16(rd + t * 4096 + 1024);
            sA = MFMA(k0, qA0, z); sA = MFMA(k1, qA1p, sA); sB = MFMA(k0, qB0, z); sB = MFMA(k1, qB1p, sB); }
        v0 = ldsr16(rd + t * 4096 + 2048); v1 = ldsr16(rd + t * 4096 + 3072);
        EXP16(sA);
        oA = MFMA(v0, pack8(sA, 0), oA); oA = MFMA(v1, pack8(sA, 1), oA);
        EXP16(sB);
        oB = MFMA(v0, pack8(sB, 0), oB); oB = MFMA(v1, pack8(sB, 1), oB);
    }
    WAITV(8); BAR();
    k0 = ldsr16(rd + A_CH); k1 = ldsr16(rd + A_CH + 1024);
    sA = MFMA(k0, qA0, z); sA = MFMA(k1, qA1p, sA); sB = MFMA(k0, qB0, z); sB = MFMA(k1, qB1p, sB);
    v0 = ldsr16(rd + A_CH + 2048); v1 = ldsr16(rd + A_CH + 3072);
    k0 = ldsr16(rd + A_CH + 4096); k1 = ldsr16(rd + A_CH + 4096 + 1024);
#pragma unroll
    for (int c = 1; c < 8; ++c) {
        const lds_cptr cb = rd + (c & 3) * A_CH, cn = rd + ((c + 1) & 3) * A_CH;
#pragma unroll
        for (int t = 0; t < 4; ++t) {
            const bool last = (c == 7 && t == 3);
            if (t == 2 && c < 7) {
                if (c < 6) WAITV(4); else WAITV(0);
                BAR();
                if (c + 3 < 8) A_STAGE(c + 3);
                if (act3) { if (c == 5) WO_STAGE1(); if (c == 6) WO_STAGE2(); }
            }
            if (!last) { sAn = MFMA(k0, qA0, z); sAn = MFMA(k1, qA1p, sAn); sBn = MFMA(k0, qB0, z); sBn = MFMA(k1, qB1p, sBn); }
            if (!last) { const lds_cptr vp = (t < 3) ? cb + (t + 1) * 4096 : cn; v0n = ldsr16(vp + 2048); v1n = ldsr16(vp + 3072); }
            if (!(c == 7 && t >= 2)) { const lds_cptr kp = (t < 2) ? cb + (t + 2) * 4096 : cn + (t - 2) * 4096; k0 = ldsr16(kp); k1 = ldsr16(kp + 1024); }
            EXP16(sA);
            oA = MFMA(v0, pack8(sA, 0), oA); oA = MFMA(v1, pack8(sA, 1), oA);
            EXP16(sB);
            oB = MFMA(v0, pack8(sB, 0), oB); oB = MFMA(v1, pack8(sB, 1), oB);
            sA = sAn; sB = sBn; v0 = v0n; v1 = v1n;
        }
    }
    __attribute__((address_space(3))) int* flags = (__attribute__((address_space(3))) int*)(lds + F_FLAGS);
    const bool bad = h && !(fmaxf(oA[12], oB[12]) < 1.0e24f && oA[12] == oA[12] && oB[12] == oB[12]);
    const int anybad = __any(bad);
    if (lane == 0) flags[w] = anybad;
    BAR();
    typedef __attribute__((ext_vector_type(4))) float f32x4;
    __attribute__((address_space(3))) f32x4* xw = (__attribute__((address_space(3))) f32x4*)(lds + 32768 + (qg * 2 + kh) * 4096);
    const __attribute__((address_space(3))) f32x4* xr = (const __attribute__((address_space(3))) f32x4*)(lds + 32768 + (qg * 2 + (kh ^ 1)) * 4096);
    __attribute__((address_space(3))) float* mxw = (__attribute__((address_space(3))) float*)(lds + F_MX) + (qg * 2 + kh) * 64;
    const __attribute__((address_space(3))) float* mxr = (const __attribute__((address_space(3))) float*)(lds + F_MX) + (qg * 2 + (kh ^ 1)) * 64;
#define A_PUT() do { const f32x16& og = kh ? oA : oB; _Pragma("unroll") for (int g = 0; g < 4; ++g) xw[g * 64 + lane] = f32x4{og[4 * g], og[4 * g + 1], og[4 * g + 2], og[4 * g + 3]}; \
        mxw[lane] = kh ? mA : mB; } while (0)
    A_PUT();
    const int redo = __any(flags[lane & 7]);
    BAR();
    if (redo) {
        BAR();
        WAITV(0);
        attn_safe(kvh, qblk, dst, rd, qA0, qA1, qB0, qB1, oA, oB, mA, mB);
        BAR();
        A_PUT();
        if (act3) { WO_STAGE1(); WO_STAGE2(); }
        BAR();
    }
    {
        f32x16 o = kh ? oB : oA; const float m = kh ? mB : mA;
        const float m2 = mxr[lane], mn = fmaxf(m, m2), a1 = __builtin_amdgcn_exp2f(m - mn), a2 = __builtin_amdgcn_exp2f(m2 - mn);
#pragma unroll
        for (int g = 0; g < 4; ++g) { const f32x4 t = xr[g * 64 + lane];
#pragma unroll
            for (int j = 0; j < 4; ++j) o[4 * g + j] = a1 * o[4 * g + j] + a2 * t[j]; }
        const float lo = swap_halves(o[12]);
        const float l = h ? o[12] : lo, inv = 1.0f / l;
#pragma unroll
        for (int r = 0; r < 16; ++r) o[r] *= inv;
        if (h) {
#pragma unroll
            for (int r = 12; r < 16; ++r) o[r] = 0.f;
        }
        char* od = ws + OF_OFF + ((size_t)(((slot >> 4) * 64 + T0 + kh) * 16 + (slot & 15))) * 2048 + lane * 16;
        st16(od, pack8(o, 0)); st16(od + 1024, pack8(o, 1));
    }
    unsigned long long* oflags = (unsigned long long*)(ws + FLAG_OFF + 4096) + ((slot >> 4) * 8 + qblk) * 16;
    WAITV(0); BAR();
    if (tid == 0) __hip_atomic_store(oflags + head, token, __ATOMIC_RELAXED, __HIP_MEMORY_SCOPE_AGENT);
    if (!act3) return;
    {
        const unsigned long long* of3 = (const unsigned long long*)(ws + FLAG_OFF + 4096) + (quad >> 1) * 16 + (w >> 2) * 8;
        for (unsigned spins = 0; spins < (1u << 16); ++spins) {
            const unsigned long long f = __hip_atomic_load(of3 + (lane & 7), __ATOMIC_RELAXED, __HIP_MEMORY_SCOPE_AGENT);
            if (__all(f == token)) break;
            __builtin_amdgcn_s_sleep(2);
        }
    }
    asm volatile("" ::: "memory");
    {
        const int tl = w & 3, hh = w >> 2, T = (quad & 15) * 4 + tl;
        const auto rs = __builtin_amdgcn_make_buffer_rsrc((void*)(ws + OF_OFF), 0, 4 << 20, 0x00020000);
        const int obase = ((p3 * 64 + T) * 16 + hh * 8) * 2048 + lane * 16;
        bf16x8 of[16];
#pragma unroll
        for (int i = 0; i < 16; ++i) of[i] = __builtin_bit_cast(bf16x8, __builtin_amdgcn_raw_buffer_load_b128(rs, obase + i * 1024, 0, 16));
        const int q = (lane & 15) >> 2, cb = ((lane >> 4) & 1) * 32 + (lane & 3) * 8, kb = hh * 224 + 4 * h + q;
        f32x16 acc0 = {}, acc1 = {};
#pragma unroll
        for (int hd = 0; hd < 8; ++hd) {
#pragma unroll
            for (int s = 0; s < 2; ++s) {
                s16x4 f0[2], f1[2];
#pragma unroll
                for (int r = 0; r < 2; ++r) { int k = kb + hd * 28 + 16 * s + 8 * r; if (hd == 7 && s == 1 && r == 1) k = min(k, 447);
                    const lds_cptr rowp = lds + k * 128 + ((k >> 8) << 15) + cb; const int sw = ((k >> 1) & 1) * 64;
                    f0[r] = ldstr(rowp + sw); f1[r] = ldstr(rowp + (sw ^ 64)); }
                acc0 = MFMA(cat4(f0[0], f0[1]), of[hd * 2 + s], acc0); acc1 = MFMA(cat4(f1[0], f1[1]), of[hd * 2 + s], acc1);
            }
        }
        const f32x16& give = hh ? acc0 : acc1; f32x16 keep = hh ? acc1 : acc0;
#pragma unroll
        for (int g = 0; g < 4; ++g) xw[g * 64 + lane] = f32x4{give[4 * g], give[4 * g + 1], give[4 * g + 2], give[4 * g + 3]};
        BAR();
        __hip_bfloat16* od = out + (size_t)(T * 32 + r32) * 896 + p3 * 448 + nb * 64 + hh * 32 + 4 * h;
#pragma unroll
        for (int g = 0; g < 4; ++g) { const f32x4 t = xr[g * 64 + lane];
            u32x2 v = {pk2(keep[4 * g] + t[0], keep[4 * g + 1] + t[1]), pk2(keep[4 * g + 2] + t[2], keep[4 * g + 3] + t[3])}; *(u32x2*)(od + 8 * g) = v; }
    }
}

extern "C" void kernel_launch(void* const* d_in, const int* in_sizes, int n_in, void* d_out, int out_size, void* d_ws, size_t ws_size, hipStream_t stream) {
    const __hip_bfloat16* xA = (const __hip_bfloat16*)d_in[0]; const __hip_bfloat16* wA = (const __hip_bfloat16*)d_in[1]; const __hip_bfloat16* woA = (const __hip_bfloat16*)d_in[2];
    const __hip_bfloat16* xB = (const __hip_bfloat16*)d_in[3]; const __hip_bfloat16* wB = (const __hip_bfloat16*)d_in[4]; const __hip_bfloat16* woB = (const __hip_bfloat16*)d_in[5];
    char* ws = (char*)d_ws;
    fused_kernel<<<256, 512, 0, stream>>>(xA, wA, woA, xB, wB, woB, ws, (__hip_bfloat16*)d_out);
}
```

```cpp
#include <hip/hip_runtime.h>
#include <hip/hip_bf16.h>
#include <stdint.h>

typedef __attribute__((ext_vector_type(8))) short bf16x8;
typedef __attribute__((ext_vector_type(4))) short s16x4;
typedef __attribute__((ext_vector_type(16))) float f32x16;
typedef __attribute__((ext_vector_type(4))) unsigned u32x4;
typedef __attribute__((ext_vector_type(2))) unsigned u32x2;
typedef __attribute__((ext_vector_type(2))) float f32x2;
typedef __attribute__((ext_vector_type(2))) __bf16 b16x2;
typedef __attribute__((address_space(3))) char* lds_ptr;
typedef __attribute__((address_space(3))) const char* lds_cptr;

#define DI __device__ __forceinline__
#define MFMA(a, b, c) __builtin_amdgcn_mfma_f32_32x32x16_bf16((a), (b), (c), 0, 0, 0)

constexpr int SEQ = 2048, DM = 448, NH = 16, DH = 28, N3 = 1344, NTILE = 64;
constexpr size_t QF_OFF = 0;
constexpr size_t KV_OFF = (size_t)4 << 20;
constexpr size_t OF_OFF = (size_t)12 << 20;
constexpr size_t FLAG_OFF = (size_t)16 << 20;
extern "C" __device__ unsigned long long hw_dispatch_id() __asm("llvm.amdgcn.dispatch.id");
constexpr float QSCALE = 0.18898223650461362f * 1.4426950408889634f;

DI unsigned pk2(float a, float b) { f32x2 t = {a, b}; b16x2 r = __builtin_convertvector(t, b16x2); return __builtin_bit_cast(unsigned, r); }
DI bf16x8 pack8(const f32x16& x, int s) {
    u32x4 p = {pk2(x[8 * s], x[8 * s + 1]), pk2(x[8 * s + 2], x[8 * s + 3]), pk2(x[8 * s + 4], x[8 * s + 5]), pk2(x[8 * s + 6], x[8 * s + 7])};
    return __builtin_bit_cast(bf16x8, p);
}
DI void glds16(const void* g, lds_ptr l) {
    const unsigned base = __builtin_amdgcn_readfirstlane((unsigned)(uintptr_t)l); unsigned sv;
    asm volatile("s_mov_b32 %0, m0\n\ts_mov_b32 m0, %2\n\ts_nop 0\n\tglobal_load_lds_dwordx4 %1, off\n\ts_mov_b32 m0, %0" : "=&s"(sv) : "v"(g), "s"(base) : "memory");
}
DI void glds16c(const void* g, lds_ptr l) {
    const unsigned base = __builtin_amdgcn_readfirstlane((unsigned)(uintptr_t)l); unsigned sv;
    asm volatile("s_mov_b32 %0, m0\n\ts_mov_b32 m0, %2\n\ts_nop 0\n\tglobal_load_lds_dwordx4 %1, off sc1\n\ts_mov_b32 m0, %0" : "=&s"(sv) : "v"(g), "s"(base) : "memory");
}
DI bf16x8 ldsr16(lds_cptr p) { return *(const __attribute__((address_space(3))) bf16x8*)p; }
DI s16x4 ldstr(lds_cptr p) { return __builtin_bit_cast(s16x4, __builtin_amdgcn_ds_read_tr16_b64_v4i16((__attribute__((address_space(3))) s16x4*)p)); }
DI bf16x8 cat4(s16x4 a, s16x4 b) { return __builtin_shufflevector(a, b, 0, 1, 2, 3, 4, 5, 6, 7); }
#define STMODE 1
DI void st16(void* p, bf16x8 v) {
    asm volatile("global_store_dwordx4 %0, %1, off sc1\n\ts_nop 1" :: "v"(p), "v"(v) : "memory");
}
#define WAITV(n) asm volatile("s_waitcnt vmcnt(" #n ")" ::: "memory")
#define BAR() do { asm volatile("s_waitcnt lgkmcnt(0)" ::: "memory"); __builtin_amdgcn_s_barrier(); asm volatile("" ::: "memory"); } while (0)
DI float swap_halves(float v) {
    auto r = __builtin_amdgcn_permlane32_swap(__float_as_uint(v), __float_as_uint(v), false, false);
    return (threadIdx.x & 32) ? __uint_as_float(r[0]) : __uint_as_float(r[1]);
}

constexpr int F_XB = 32768, F_WB = 12288, F_STG = F_XB + F_WB;
constexpr int A_CH = 16384, A_GRP = 4 * A_CH, F_FLAGS = 3 * F_STG, F_MX = F_FLAGS + 256, F_LDS = F_MX + 2048;
DI float rowmax16(const f32x16& s) {
    float t = fmaxf(s[0], s[1]);
#pragma unroll
    for (int r = 2; r < 16; r += 2) t = fmaxf(fmaxf(t, s[r]), s[r + 1]);
    return fmaxf(t, swap_halves(t));
}
#define A_STAGE(c) do { const char* s_ = kvh + (size_t)((((qblk + (c)) & 7) * 8) * 4096); _Pragma("unroll") for (int i_ = 0; i_ < 4; ++i_) glds16c(s_ + i_ * 1024, dst + ((c) & 3) * A_CH + i_ * 1024); } while (0)
#define EXP16(s) do { _Pragma("unroll") for (int r_ = 0; r_ < 16; ++r_) s[r_] = __builtin_amdgcn_exp2f(s[r_]); } while (0)

DI void attn_safe(const char* __restrict__ kvh, int qblk, lds_ptr dst, lds_cptr rd, bf16x8 qa0, bf16x8 qa1, bf16x8 qb0, bf16x8 qb1, f32x16& oA, f32x16& oB, float& mA, float& mB) {
    A_STAGE(0); A_STAGE(1); A_STAGE(2);
    oA = f32x16{}; oB = f32x16{}; mA = -INFINITY; mB = -INFINITY;
    for (int c = 0; c < 8; ++c) {
        if (c < 6) WAITV(8); else if (c == 6) WAITV(4); else WAITV(0);
        BAR();
        if (c + 3 < 8) A_STAGE(c + 3);
        const lds_cptr cb = rd + (c & 3) * A_CH;
        for (int t = 0; t < 4; ++t) {
            const bf16x8 k0 = ldsr16(cb + t * 4096), k1 = ldsr16(cb + t * 4096 + 1024), v0 = ldsr16(cb + t * 4096 + 2048), v1 = ldsr16(cb + t * 4096 + 3072);
            const f32x16 z = {};
            f32x16 s = MFMA(k0, qa0, z); s = MFMA(k1, qa1, s);
            float mn = fmaxf(mA, rowmax16(s)), al = __builtin_amdgcn_exp2f(mA - mn); mA = mn;
#pragma unroll
            for (int r = 0; r < 16; ++r) { oA[r] *= al; s[r] = __builtin_amdgcn_exp2f(s[r] - mn); }
            oA = MFMA(v0, pack8(s, 0), oA); oA = MFMA(v1, pack8(s, 1), oA);
            s = MFMA(k0, qb0, z); s = MFMA(k1, qb1, s);
            mn = fmaxf(mB, rowmax16(s)); al = __builtin_amdgcn_exp2f(mB - mn); mB = mn;
#pragma unroll
            for (int r = 0; r < 16; ++r) { oB[r] *= al; s[r] = __builtin_amdgcn_exp2f(s[r] - mn); }
            oB = MFMA(v0, pack8(s, 0), oB); oB = MFMA(v1, pack8(s, 1), oB);
        }
    }
}

__global__ __launch_bounds__(512) void fused_kernel(const __hip_bfloat16* __restrict__ xA, const __hip_bfloat16* __restrict__ wA, const __hip_bfloat16* __restrict__ woA,
                                                    const __hip_bfloat16* __restrict__ xB, const __hip_bfloat16* __restrict__ wB, const __hip_bfloat16* __restrict__ woB,
                                                    char* __restrict__ ws, __hip_bfloat16* __restrict__ out) {
    __shared__ __attribute__((aligned(16))) char smem[F_LDS];
    const lds_ptr lds = (lds_ptr)smem;
    const int tid = threadIdx.x, lane = tid & 63, w = __builtin_amdgcn_readfirstlane(tid >> 6);
    const int h = lane >> 5, r32 = lane & 31;
    const int bid = blockIdx.x, xcd = bid & 7, idx = bid >> 3, slot = xcd + 8 * (idx >> 3), qblk = idx & 7;
    const int head = slot & 15;
    {
        const __hip_bfloat16* x = (slot >> 4) ? xB : xA; const __hip_bfloat16* W = (slot >> 4) ? wB : wA;
        const __hip_bfloat16* xsrc[4]; const __hip_bfloat16* wsrc[2];
#pragma unroll
        for (int i = 0; i < 4; ++i) { const int row = 32 * w + 8 * i + (lane >> 3), c = (lane & 7) ^ ((row >> 1) & 7); xsrc[i] = x + (size_t)(qblk * 256 + row) * DM + 8 * c; }
        const int j0 = (w < 4) ? 2 * w : 4 + w;
#pragma unroll
        for (int i = 0; i < 2; ++i) { const int j = j0 + i, row = 16 * (j & 3) + (lane >> 2), pc = lane & 3;
            wsrc[i] = W + (size_t)row * N3 + (j >> 2) * DM + head * DH + (pc == 3 ? 20 : 8 * pc); }
#define F_STAGE(kc, sl) do { _Pragma("unroll") for (int i = 0; i < 4; ++i) glds16(xsrc[i] + (kc) * 64, lds + (sl) * F_STG + (4 * w + i) * 1024); \
        glds16(wsrc[0] + (size_t)(kc) * 64 * N3, lds + (sl) * F_STG + F_XB + j0 * 1024); \
        if (w < 4) glds16(wsrc[1] + (size_t)(kc) * 64 * N3, lds + (sl) * F_STG + F_XB + (j0 + 1) * 1024); } while (0)
        F_STAGE(0, 0); F_STAGE(1, 1);
        const int xrow = 32 * w + r32, xsw = (xrow >> 1) & 7, col4 = ((lane >> 4) & 1) * 4 + (lane & 3);
        const int wbase = F_XB + (8 * h + ((lane & 15) >> 2)) * 64 + (col4 <= 5 ? col4 * 8 : 56);
        f32x16 aq = {}, ak = {}, av = {};
#pragma unroll
        for (int kc = 0; kc < 7; ++kc) {
            if (kc < 6) { if (w < 4) WAITV(6); else WAITV(5); } else WAITV(0);
            BAR();
            if (kc + 2 < 7) F_STAGE(kc + 2, (kc + 2) % 3);
            const lds_cptr xs = lds + (kc % 3) * F_STG, wsb = xs + wbase;
#pragma unroll
            for (int ks = 0; ks < 4; ++ks) {
                const bf16x8 xf = ldsr16(xs + xrow * 128 + (((2 * ks + h) ^ xsw) * 16));
                const bf16x8 wq = cat4(ldstr(wsb + ks * 1024), ldstr(wsb + ks * 1024 + 256));
                const bf16x8 wk = cat4(ldstr(wsb + ks * 1024 + 4096), ldstr(wsb + ks * 1024 + 4096 + 256));
                const bf16x8 wv = cat4(ldstr(wsb + ks * 1024 + 8192), ldstr(wsb + ks * 1024 + 8192 + 256));
                aq = MFMA(wq, xf, aq); ak = MFMA(wk, xf, ak); av = MFMA(xf, wv, av);
            }
        }
#undef F_STAGE
#pragma unroll
        for (int r = 0; r < 16; ++r) aq[r] *= QSCALE;
        if (h) {
#pragma unroll
            for (int r = 12; r < 16; ++r) { aq[r] = 0.f; ak[r] = 0.f; }
            ak[12] = 1.0f;
        }
        if (r32 >= 28) { const float f = (r32 == 28) ? 1.f : 0.f;
#pragma unroll
            for (int r = 0; r < 16; ++r) av[r] = f; }
        const bf16x8 fk0 = pack8(ak, 0), fk1 = pack8(ak, 1), fv0 = pack8(av, 0), fv1 = pack8(av, 1);
        char* kd = ws + KV_OFF + ((size_t)(slot * 64 + qblk * 8 + w)) * 4096 + lane * 16;
        st16(kd, fk0); st16(kd + 1024, fk1); st16(kd + 2048, fv0); st16(kd + 3072, fv1);
        BAR();
        typedef __attribute__((address_space(3))) bf16x8* lds_frag;
        lds_frag kl = (lds_frag)(lds + (w >> 2) * A_GRP + (w & 3) * 4096 + lane * 16);
        kl[0] = fk0; kl[64] = fk1; kl[128] = fv0; kl[192] = fv1;
        lds_frag ql = (lds_frag)(lds + 3 * A_CH + w * 2048 + lane * 16);
        ql[0] = pack8(aq, 0); ql[64] = pack8(aq, 1);
        BAR();
    }
    const int qg = w & 3, kh = w >> 2, T0 = qblk * 8 + 2 * qg;
    const char* kvh = ws + KV_OFF + ((size_t)(slot * 64 + kh * 4 + qg)) * 4096 + lane * 16;
    const lds_ptr dst = lds + kh * A_GRP + qg * 4096;
    const lds_cptr rd = lds + kh * A_GRP + lane * 16;
    const bf16x8 qA0 = ldsr16(lds + 3 * A_CH + qg * 4096 + lane * 16), qA1 = ldsr16(lds + 3 * A_CH + qg * 4096 + 1024 + lane * 16);
    const bf16x8 qB0 = ldsr16(lds + 3 * A_CH + qg * 4096 + 2048 + lane * 16), qB1 = ldsr16(lds + 3 * A_CH + qg * 4096 + 3072 + lane * 16);
    const f32x16 z = {};
    f32x16 oA = {}, oB = {}, sA, sB, sAn, sBn;
    float mA, mB;
    bf16x8 k0 = ldsr16(rd), k1 = ldsr16(rd + 1024), v0, v1, v0n, v1n;
    {
        sA = MFMA(k0, qA0, z); sA = MFMA(k1, qA1, sA); sB = MFMA(k0, qB0, z); sB = MFMA(k1, qB1, sB);
        const unsigned pm = pk2(-rowmax16(sA), -rowmax16(sB));
        mA = -__uint_as_float(pm << 16); mB = -__uint_as_float(pm & 0xffff0000u);
#pragma unroll
        for (int r = 0; r < 16; ++r) { sA[r] -= mA; sB[r] -= mB; }
    }
    bf16x8 qA1p = qA1, qB1p = qB1;
    if (h) { qA1p[4] = (short)(__float_as_uint(-mA) >> 16); qB1p[4] = (short)(__float_as_uint(-mB) >> 16); }
    const unsigned long long token = (hw_dispatch_id() * 0x9E3779B97F4A7C15ull) ^ ((unsigned long long)(uintptr_t)__builtin_amdgcn_dispatch_ptr() << 1) ^ 0x5851F42D4C957F2Dull;
    unsigned long long* flags_g = (unsigned long long*)(ws + FLAG_OFF) + slot * 8;
    const bool act3 = idx < 28; const int quad = 4 * xcd + idx / 7, nb = idx % 7, p3 = quad >> 4;
    const __hip_bfloat16* wo3 = (p3 ? woB : woA) + nb * 64 + (((lane & 7) >> 2) ^ ((lane >> 4) & 1)) * 32 + (lane & 3) * 8 + (size_t)(lane >> 3) * DM;
#define WO_INS(ii) glds16(wo3 + (size_t)(ii) * 8 * DM, lds + (ii) * 1024 + ((ii) >= 32 ? 32768 : 0))
#define WO_STAGE1() do { _Pragma("unroll") for (int i_ = 0; i_ < 2; ++i_) { WO_INS(2 * w + i_); WO_INS(32 + 2 * w + i_); } } while (0)
#define WO_STAGE2() do { _Pragma("unroll") for (int i_ = 0; i_ < 2; ++i_) WO_INS(16 + 2 * w + i_); WO_INS(48 + w); } while (0)
    v0 = ldsr16(rd + 2048); v1 = ldsr16(rd + 3072);
    k0 = ldsr16(rd + 4096); k1 = ldsr16(rd + 4096 + 1024);
#pragma unroll
    for (int t = 0; t < 4; ++t) {
        if (t == 1) {
            WAITV(0); BAR();
            if (tid == 0) __hip_atomic_store(flags_g + qblk, token, __ATOMIC_RELAXED, __HIP_MEMORY_SCOPE_AGENT);
        }
        if (t == 2) {
            for (unsigned spins = 0; spins < (1u << 16); ++spins) {
                const unsigned long long f = __hip_atomic_load(flags_g + (lane & 7), __ATOMIC_RELAXED, __HIP_MEMORY_SCOPE_AGENT);
                if (__all(f == token)) break;
                __builtin_amdgcn_s_sleep(2);
            }
            A_STAGE(1); A_STAGE(2); A_STAGE(3);
        }
        if (t < 3) { sAn = MFMA(k0, qA0, z); sAn = MFMA(k1, qA1p, sAn); sBn = MFMA(k0, qB0, z); sBn = MFMA(k1, qB1p, sBn);
            v0n = ldsr16(rd + (t + 1) * 4096 + 2048); v1n = ldsr16(rd + (t + 1) * 4096 + 3072); }
        if (t < 2) { k0 = ldsr16(rd + (t + 2) * 4096); k1 = ldsr16(rd + (t + 2) * 4096 + 1024); }
        EXP16(sA);
        oA = MFMA(v0, pack8(sA, 0), oA); oA = MFMA(v1, pack8(sA, 1), oA);
        EXP16(sB);
        oB = MFMA(v0, pack8(sB, 0), oB); oB = MFMA(v1, pack8(sB, 1), oB);
        if (t < 3) { sA = sAn; sB = sBn; v0 = v0n; v1 = v1n; }
    }
    WAITV(8); BAR();
    k0 = ldsr16(rd + A_CH); k1 = ldsr16(rd + A_CH + 1024);
    sA = MFMA(k0, qA0, z); sA = MFMA(k1, qA1p, sA); sB = MFMA(k0, qB0, z); sB = MFMA(k1, qB1p, sB);
    v0 = ldsr16(rd + A_CH + 2048); v1 = ldsr16(rd + A_CH + 3072);
    k0 = ldsr16(rd + A_CH + 4096); k1 = ldsr16(rd + A_CH + 4096 + 1024);
#pragma unroll
    for (int c = 1; c < 8; ++c) {
        const lds_cptr cb = rd + (c & 3) * A_CH, cn = rd + ((c + 1) & 3) * A_CH;
#pragma unroll
        for (int t = 0; t < 4; ++t) {
            const bool last = (c == 7 && t == 3);
            if (t == 2 && c < 7) {
                if (c < 6) WAITV(4); else WAITV(0);
                BAR();
                if (c + 3 < 8) A_STAGE(c + 3);
                if (act3) { if (c == 5) WO_STAGE1(); if (c == 6) WO_STAGE2(); }
            }
            if (!last) { sAn = MFMA(k0, qA0, z); sAn = MFMA(k1, qA1p, sAn); sBn = MFMA(k0, qB0, z); sBn = MFMA(k1, qB1p, sBn); }
            if (!last) { const lds_cptr vp = (t < 3) ? cb + (t + 1) * 4096 : cn; v0n = ldsr16(vp + 2048); v1n = ldsr16(vp + 3072); }
            if (!(c == 7 && t >= 2)) { const lds_cptr kp = (t < 2) ? cb + (t + 2) * 4096 : cn + (t - 2) * 4096; k0 = ldsr16(kp); k1 = ldsr16(kp + 1024); }
            EXP16(sA);
            oA = MFMA(v0, pack8(sA, 0), oA); oA = MFMA(v1, pack8(sA, 1), oA);
            EXP16(sB);
            oB = MFMA(v0, pack8(sB, 0), oB); oB = MFMA(v1, pack8(sB, 1), oB);
            sA = sAn; sB = sBn; v0 = v0n; v1 = v1n;
        }
    }
    __attribute__((address_space(3))) int* flags = (__attribute__((address_space(3))) int*)(lds + F_FLAGS);
    const bool bad = h && !(fmaxf(oA[12], oB[12]) < 1.0e24f && oA[12] == oA[12] && oB[12] == oB[12]);
    const int anybad = __any(bad);
    if (lane == 0) flags[w] = anybad;
    BAR();
    typedef __attribute__((ext_vector_type(4))) float f32x4;
    __attribute__((address_space(3))) f32x4* xw = (__attribute__((address_space(3))) f32x4*)(lds + 32768 + (qg * 2 + kh) * 4096);
    const __attribute__((address_space(3))) f32x4* xr = (const __attribute__((address_space(3))) f32x4*)(lds + 32768 + (qg * 2 + (kh ^ 1)) * 4096);
    __attribute__((address_space(3))) float* mxw = (__attribute__((address_space(3))) float*)(lds + F_MX) + (qg * 2 + kh) * 64;
    const __attribute__((address_space(3))) float* mxr = (const __attribute__((address_space(3))) float*)(lds + F_MX) + (qg * 2 + (kh ^ 1)) * 64;
#define A_PUT() do { const f32x16& og = kh ? oA : oB; _Pragma("unroll") for (int g = 0; g < 4; ++g) xw[g * 64 + lane] = f32x4{og[4 * g], og[4 * g + 1], og[4 * g + 2], og[4 * g + 3]}; \
        mxw[lane] = kh ? mA : mB; } while (0)
    A_PUT();
    const int redo = __any(flags[lane & 7]);
    BAR();
    if (redo) {
        BAR();
        WAITV(0);
        attn_safe(kvh, qblk, dst, rd, qA0, qA1, qB0, qB1, oA, oB, mA, mB);
        BAR();
        A_PUT();
        if (act3) { WO_STAGE1(); WO_STAGE2(); }
        BAR();
    }
    {
        f32x16 o = kh ? oB : oA; const float m = kh ? mB : mA;
        const float m2 = mxr[lane], mn = fmaxf(m, m2), a1 = __builtin_amdgcn_exp2f(m - mn), a2 = __builtin_amdgcn_exp2f(m2 - mn);
#pragma unroll
        for (int g = 0; g < 4; ++g) { const f32x4 t = xr[g * 64 + lane];
#pragma unroll
            for (int j = 0; j < 4; ++j) o[4 * g + j] = a1 * o[4 * g + j] + a2 * t[j]; }
        const float lo = swap_halves(o[12]);
        const float l = h ? o[12] : lo, inv = 1.0f / l;
#pragma unroll
        for (int r = 0; r < 16; ++r) o[r] *= inv;
        if (h) {
#pragma unroll
            for (int r = 12; r < 16; ++r) o[r] = 0.f;
        }
        char* od = ws + OF_OFF + ((size_t)(((slot >> 4) * 64 + T0 + kh) * 16 + (slot & 15))) * 2048 + lane * 16;
        st16(od, pack8(o, 0)); st16(od + 1024, pack8(o, 1));
    }
    unsigned long long* oflags = (unsigned long long*)(ws + FLAG_OFF + 4096) + ((slot >> 4) * 8 + qblk) * 16;
    WAITV(0); BAR();
    if (tid == 0) __hip_atomic_store(oflags + head, token, __ATOMIC_RELAXED, __HIP_MEMORY_SCOPE_AGENT);
    if (!act3) return;
    {
        const int tl = w & 3, hh = w >> 2, T = (quad & 15) * 4 + tl;
        const int q = (lane & 15) >> 2, cb = ((lane >> 4) & 1) * 32 + (lane & 3) * 8, kb = hh * 224 + 4 * h + q;
        bf16x8 wf0[16], wf1[16];
#pragma unroll
        for (int i = 0; i < 16; ++i) {
            s16x4 f0[2], f1[2];
#pragma unroll
            for (int r = 0; r < 2; ++r) { int k = kb + (i >> 1) * 28 + 16 * (i & 1) + 8 * r; if (i == 15 && r == 1) k = min(k, 447);
                const lds_cptr rowp = lds + k * 128 + ((k >> 8) << 15) + cb; const int sw = ((k >> 1) & 1) * 64;
                f0[r] = ldstr(rowp + sw); f1[r] = ldstr(rowp + (sw ^ 64)); }
            wf0[i] = cat4(f0[0], f0[1]); wf1[i] = cat4(f1[0], f1[1]);
        }
        {
            const unsigned long long* of3 = (const unsigned long long*)(ws + FLAG_OFF + 4096) + (quad >> 1) * 16 + hh * 8;
            for (unsigned spins = 0; spins < (1u << 16); ++spins) {
                const unsigned long long f = __hip_atomic_load(of3 + (lane & 7), __ATOMIC_RELAXED, __HIP_MEMORY_SCOPE_AGENT);
                if (__all(f == token)) break;
                __builtin_amdgcn_s_sleep(2);
            }
        }
        asm volatile("" ::: "memory");
        const auto rs = __builtin_amdgcn_make_buffer_rsrc((void*)(ws + OF_OFF), 0, 4 << 20, 0x00020000);
        const int obase = ((p3 * 64 + T) * 16 + hh * 8) * 2048 + lane * 16;
        bf16x8 of[16];
#pragma unroll
        for (int i = 0; i < 16; ++i) of[i] = __builtin_bit_cast(bf16x8, __builtin_amdgcn_raw_buffer_load_b128(rs, obase + i * 1024, 0, 16));
        f32x16 acc0 = {}, acc1 = {};
#pragma unroll
        for (int i = 0; i < 16; ++i) { acc0 = MFMA(wf0[i], of[i], acc0); acc1 = MFMA(wf1[i], of[i], acc1); }
        const f32x16& give = hh ? acc0 : acc1; f32x16 keep = hh ? acc1 : acc0;
#pragma unroll
        for (int g = 0; g < 4; ++g) xw[g * 64 + lane] = f32x4{give[4 * g], give[4 * g + 1], give[4 * g + 2], give[4 * g + 3]};
        BAR();
        u32x2 pk[4];
#pragma unroll
        for (int g = 0; g < 4; ++g) { const f32x4 t = xr[g * 64 + lane];
            pk[g] = u32x2{pk2(keep[4 * g] + t[0], keep[4 * g + 1] + t[1]), pk2(keep[4 * g + 2] + t[2], keep[4 * g + 3] + t[3])}; }
        __hip_bfloat16* od = out + (size_t)(T * 32 + r32) * 896 + p3 * 448 + nb * 64 + hh * 32 + 8 * h;
#pragma unroll
        for (int g = 0; g < 4; g += 2) {
            const auto rx = __builtin_amdgcn_permlane32_swap(pk[g][0], pk[g + 1][0], false, false);
            const auto ry = __builtin_amdgcn_permlane32_swap(pk[g][1], pk[g + 1][1], false, false);
            *(u32x4*)(od + 8 * g) = u32x4{rx[0], ry[0], rx[1], ry[1]};
        }
    }
}

extern "C" void kernel_launch(void* const* d_in, const int* in_sizes, int n_in, void* d_out, int out_size, void* d_ws, size_t ws_size, hipStream_t stream) {
    const __hip_bfloat16* xA = (const __hip_bfloat16*)d_in[0]; const __hip_bfloat16* wA = (const __hip_bfloat16*)d_in[1]; const __hip_bfloat16* woA = (const __hip_bfloat16*)d_in[2];
    const __hip_bfloat16* xB = (const __hip_bfloat16*)d_in[3]; const __hip_bfloat16* wB = (const __hip_bfloat16*)d_in[4]; const __hip_bfloat16* woB = (const __hip_bfloat16*)d_in[5];
    char* ws = (char*)d_ws;
    fused_kernel<<<256, 512, 0, stream>>>(xA, wA, woA, xB, wB, woB, ws, (__hip_bfloat16*)d_out);
}
```

```cpp
#include <hip/hip_runtime.h>
#include <hip/hip_bf16.h>
#include <stdint.h>

typedef __attribute__((ext_vector_type(8))) short bf16x8;
typedef __attribute__((ext_vector_type(4))) short s16x4;
typedef __attribute__((ext_vector_type(16))) float f32x16;
typedef __attribute__((ext_vector_type(4))) unsigned u32x4;
typedef __attribute__((ext_vector_type(2))) unsigned u32x2;
typedef __attribute__((ext_vector_type(2))) float f32x2;
typedef __attribute__((ext_vector_type(2))) __bf16 b16x2;
typedef __attribute__((address_space(3))) char* lds_ptr;
typedef __attribute__((address_space(3))) const char* lds_cptr;

#define DI __device__ __forceinline__
#define MFMA(a, b, c) __builtin_amdgcn_mfma_f32_32x32x16_bf16((a), (b), (c), 0, 0, 0)

constexpr int DM = 448, DH = 28, N3 = 1344;
constexpr size_t KV_OFF = (size_t)4 << 20;
constexpr size_t OF_OFF = (size_t)12 << 20;
constexpr size_t FLAG_OFF = (size_t)16 << 20;
extern "C" __device__ unsigned long long hw_dispatch_id() __asm("llvm.amdgcn.dispatch.id");
constexpr float QSCALE = 0.18898223650461362f * 1.4426950408889634f;

DI unsigned pk2(float a, float b) { f32x2 t = {a, b}; b16x2 r = __builtin_convertvector(t, b16x2); return __builtin_bit_cast(unsigned, r); }
DI bf16x8 pack8(const f32x16& x, int s) {
    u32x4 p = {pk2(x[8 * s], x[8 * s + 1]), pk2(x[8 * s + 2], x[8 * s + 3]), pk2(x[8 * s + 4], x[8 * s + 5]), pk2(x[8 * s + 6], x[8 * s + 7])};
    return __builtin_bit_cast(bf16x8, p);
}
DI void glds16(const void* g, lds_ptr l) {
    const unsigned base = __builtin_amdgcn_readfirstlane((unsigned)(uintptr_t)l); unsigned sv;
    asm volatile("s_mov_b32 %0, m0\n\ts_mov_b32 m0, %2\n\ts_nop 0\n\tglobal_load_lds_dwordx4 %1, off\n\ts_mov_b32 m0, %0" : "=&s"(sv) : "v"(g), "s"(base) : "memory");
}
DI void glds16c(const void* g, lds_ptr l) {
    const unsigned base = __builtin_amdgcn_readfirstlane((unsigned)(uintptr_t)l); unsigned sv;
    asm volatile("s_mov_b32 %0, m0\n\ts_mov_b32 m0, %2\n\ts_nop 0\n\tglobal_load_lds_dwordx4 %1, off sc1\n\ts_mov_b32 m0, %0" : "=&s"(sv) : "v"(g), "s"(base) : "memory");
}
DI bf16x8 ldsr16(lds_cptr p) { return *(const __attribute__((address_space(3))) bf16x8*)p; }
DI s16x4 ldstr(lds_cptr p) { return __builtin_bit_cast(s16x4, __builtin_amdgcn_ds_read_tr16_b64_v4i16((__attribute__((address_space(3))) s16x4*)p)); }
DI bf16x8 cat4(s16x4 a, s16x4 b) { return __builtin_shufflevector(a, b, 0, 1, 2, 3, 4, 5, 6, 7); }
DI void st16(void* p, bf16x8 v) {
    asm volatile("global_store_dwordx4 %0, %1, off sc1\n\ts_nop 1" :: "v"(p), "v"(v) : "memory");
}
#define WAITV(n) asm volatile("s_waitcnt vmcnt(" #n ")" ::: "memory")
#define BAR() do { asm volatile("s_waitcnt lgkmcnt(0)" ::: "memory"); __builtin_amdgcn_s_barrier(); asm volatile("" ::: "memory"); } while (0)
DI float swap_halves(float v) {
    auto r = __builtin_amdgcn_permlane32_swap(__float_as_uint(v), __float_as_uint(v), false, false);
    return (threadIdx.x & 32) ? __uint_as_float(r[0]) : __uint_as_float(r[1]);
}

constexpr int F_XB = 32768, F_WB = 12288, F_STG = F_XB + F_WB;
constexpr int A_CH = 16384, A_GRP = 4 * A_CH, F_FLAGS = 3 * F_STG, F_MX = F_FLAGS + 256, F_LDS = F_MX + 2048;
DI float rowmax16(const f32x16& s) {
    float t = fmaxf(s[0], s[1]);
#pragma unroll
    for (int r = 2; r < 16; r += 2) t = fmaxf(fmaxf(t, s[r]), s[r + 1]);
    return fmaxf(t, swap_halves(t));
}
#define A_STAGE(c) do { const char* s_ = kvh + (size_t)((((qblk + (c)) & 7) * 8) * 4096); _Pragma("unroll") for (int i_ = 0; i_ < 4; ++i_) glds16c(s_ + i_ * 1024, dst + ((c) & 3) * A_CH + i_ * 1024); } while (0)
#define EXP16(s) do { _Pragma("unroll") for (int r_ = 0; r_ < 16; ++r_) s[r_] = __builtin_amdgcn_exp2f(s[r_]); } while (0)

DI void attn_safe(const char* __restrict__ kvh, int qblk, lds_ptr dst, lds_cptr rd, bf16x8 qa0, bf16x8 qa1, bf16x8 qb0, bf16x8 qb1, f32x16& oA, f32x16& oB, float& mA, float& mB) {
    A_STAGE(0); A_STAGE(1); A_STAGE(2);
    oA = f32x16{}; oB = f32x16{}; mA = -INFINITY; mB = -INFINITY;
    for (int c = 0; c < 8; ++c) {
        if (c < 6) WAITV(8); else if (c == 6) WAITV(4); else WAITV(0);
        BAR();
        if (c + 3 < 8) A_STAGE(c + 3);
        const lds_cptr cb = rd + (c & 3) * A_CH;
        for (int t = 0; t < 4; ++t) {
            const bf16x8 k0 = ldsr16(cb + t * 4096), k1 = ldsr16(cb + t * 4096 + 1024), v0 = ldsr16(cb + t * 4096 + 2048), v1 = ldsr16(cb + t * 4096 + 3072);
            const f32x16 z = {};
            f32x16 s = MFMA(k0, qa0, z); s = MFMA(k1, qa1, s);
            float mn = fmaxf(mA, rowmax16(s)), al = __builtin_amdgcn_exp2f(mA - mn); mA = mn;
#pragma unroll
            for (int r = 0; r < 16; ++r) { oA[r] *= al; s[r] = __builtin_amdgcn_exp2f(s[r] - mn); }
            oA = MFMA(v0, pack8(s, 0), oA); oA = MFMA(v1, pack8(s, 1), oA);
            s = MFMA(k0, qb0, z); s = MFMA(k1, qb1, s);
            mn = fmaxf(mB, rowmax16(s)); al = __builtin_amdgcn_exp2f(mB - mn); mB = mn;
#pragma unroll
            for (int r = 0; r < 16; ++r) { oB[r] *= al; s[r] = __builtin_amdgcn_exp2f(s[r] - mn); }
            oB = MFMA(v0, pack8(s, 0), oB); oB = MFMA(v1, pack8(s, 1), oB);
        }
    }
}

__global__ __launch_bounds__(512) void fused_kernel(const __hip_bfloat16* __restrict__ xA, const __hip_bfloat16* __restrict__ wA, const __hip_bfloat16* __restrict__ woA,
                                                    const __hip_bfloat16* __restrict__ xB, const __hip_bfloat16* __restrict__ wB, const __hip_bfloat16* __restrict__ woB,
                                                    char* __restrict__ ws, __hip_bfloat16* __restrict__ out) {
    __shared__ __attribute__((aligned(16))) char smem[F_LDS];
    const lds_ptr lds = (lds_ptr)smem;
    const int tid = threadIdx.x, lane = tid & 63, w = __builtin_amdgcn_readfirstlane(tid >> 6);
    const int h = lane >> 5, r32 = lane & 31;
    const int bid = blockIdx.x, xcd = bid & 7, idx = bid >> 3, slot = xcd + 8 * (idx >> 3), qblk = idx & 7;
    const int head = slot & 15;
    {
        const __hip_bfloat16* x = (slot >> 4) ? xB : xA; const __hip_bfloat16* W = (slot >> 4) ? wB : wA;
        const __hip_bfloat16* xsrc[4]; const __hip_bfloat16* wsrc[2];
#pragma unroll
        for (int i = 0; i < 4; ++i) { const int row = 32 * w + 8 * i + (lane >> 3), c = (lane & 7) ^ ((row >> 1) & 7); xsrc[i] = x + (size_t)(qblk * 256 + row) * DM + 8 * c; }
        const int j0 = (w < 4) ? 2 * w : 4 + w;
#pragma unroll
        for (int i = 0; i < 2; ++i) { const int j = j0 + i, row = 16 * (j & 3) + (lane >> 2), pc = lane & 3;
            wsrc[i] = W + (size_t)row * N3 + (j >> 2) * DM + head * DH + (pc == 3 ? 20 : 8 * pc); }
#define F_STAGE(kc, sl) do { _Pragma("unroll") for (int i = 0; i < 4; ++i) glds16(xsrc[i] + (kc) * 64, lds + (sl) * F_STG + (4 * w + i) * 1024); \
        glds16(wsrc[0] + (size_t)(kc) * 64 * N3, lds + (sl) * F_STG + F_XB + j0 * 1024); \
        if (w < 4) glds16(wsrc[1] + (size_t)(kc) * 64 * N3, lds + (sl) * F_STG + F_XB + (j0 + 1) * 1024); } while (0)
        F_STAGE(0, 0); F_STAGE(1, 1);
        const int xrow = 32 * w + r32, xsw = (xrow >> 1) & 7, col4 = ((lane >> 4) & 1) * 4 + (lane & 3);
        const int wbase = F_XB + (8 * h + ((lane & 15) >> 2)) * 64 + (col4 <= 5 ? col4 * 8 : 56);
        f32x16 aq = {}, ak = {}, av = {};
#pragma unroll
        for (int kc = 0; kc < 7; ++kc) {
            if (kc < 6) { if (w < 4) WAITV(6); else WAITV(5); } else WAITV(0);
            BAR();
            if (kc + 2 < 7) F_STAGE(kc + 2, (kc + 2) % 3);
            const lds_cptr xs = lds + (kc % 3) * F_STG, wsb = xs + wbase;
#pragma unroll
            for (int ks = 0; ks < 4; ++ks) {
                const bf16x8 xf = ldsr16(xs + xrow * 128 + (((2 * ks + h) ^ xsw) * 16));
                const bf16x8 wq = cat4(ldstr(wsb + ks * 1024), ldstr(wsb + ks * 1024 + 256));
                const bf16x8 wk = cat4(ldstr(wsb + ks * 1024 + 4096), ldstr(wsb + ks * 1024 + 4096 + 256));
                const bf16x8 wv = cat4(ldstr(wsb + ks * 1024 + 8192), ldstr(wsb + ks * 1024 + 8192 + 256));
                aq = MFMA(wq, xf, aq); ak = MFMA(wk, xf, ak); av = MFMA(xf, wv, av);
            }
        }
#undef F_STAGE
#pragma unroll
        for (int r = 0; r < 16; ++r) aq[r] *= QSCALE;
        if (h) {
#pragma unroll
            for (int r = 12; r < 16; ++r) { aq[r] = 0.f; ak[r] = 0.f; }
            ak[12] = 1.0f;
        }
        if (r32 >= 28) { const float f = (r32 == 28) ? 1.f : 0.f;
#pragma unroll
            for (int r = 0; r < 16; ++r) av[r] = f; }
        const bf16x8 fk0 = pack8(ak, 0), fk1 = pack8(ak, 1), fv0 = pack8(av, 0), fv1 = pack8(av, 1);
        char* kd = ws + KV_OFF + ((size_t)(slot * 64 + qblk * 8 + w)) * 4096 + lane * 16;
        st16(kd, fk0); st16(kd + 1024, fk1); st16(kd + 2048, fv0); st16(kd + 3072, fv1);
        BAR();
        typedef __attribute__((address_space(3))) bf16x8* lds_frag;
        lds_frag kl = (lds_frag)(lds + (w >> 2) * A_GRP + (w & 3) * 4096 + lane * 16);
        kl[0] = fk0; kl[64] = fk1; kl[128] = fv0; kl[192] = fv1;
        lds_frag ql = (lds_frag)(lds + 3 * A_CH + w * 2048 + lane * 16);
        ql[0] = pack8(aq, 0); ql[64] = pack8(aq, 1);
        BAR();
    }
    const int qg = w & 3, kh = w >> 2, T0 = qblk * 8 + 2 * qg;
    const char* kvh = ws + KV_OFF + ((size_t)(slot * 64 + kh * 4 + qg)) * 4096 + lane * 16;
    const lds_ptr dst = lds + kh * A_GRP + qg * 4096;
    const lds_cptr rd = lds + kh * A_GRP + lane * 16;
    const bf16x8 qA0 = ldsr16(lds + 3 * A_CH + qg * 4096 + lane * 16), qA1 = ldsr16(lds + 3 * A_CH + qg * 4096 + 1024 + lane * 16);
    const bf16x8 qB0 = ldsr16(lds + 3 * A_CH + qg * 4096 + 2048 + lane * 16), qB1 = ldsr16(lds + 3 * A_CH + qg * 4096 + 3072 + lane * 16);
    const f32x16 z = {};
    f32x16 oA = {}, oB = {}, sA, sB, sAn, sBn;
    float mA, mB;
    bf16x8 k0 = ldsr16(rd), k1 = ldsr16(rd + 1024), v0, v1, v0n, v1n;
    {
        sA = MFMA(k0, qA0, z); sA = MFMA(k1, qA1, sA); sB = MFMA(k0, qB0, z); sB = MFMA(k1, qB1, sB);
        const unsigned pm = pk2(-rowmax16(sA), -rowmax16(sB));
        mA = -__uint_as_float(pm << 16); mB = -__uint_as_float(pm & 0xffff0000u);
#pragma unroll
        for (int r = 0; r < 16; ++r) { sA[r] -= mA; sB[r] -= mB; }
    }
    bf16x8 qA1p = qA1, qB1p = qB1;
    if (h) { qA1p[4] = (short)(__float_as_uint(-mA) >> 16); qB1p[4] = (short)(__float_as_uint(-mB) >> 16); }
    const unsigned long long token = (hw_dispatch_id() * 0x9E3779B97F4A7C15ull) ^ ((unsigned long long)(uintptr_t)__builtin_amdgcn_dispatch_ptr() << 1) ^ 0x5851F42D4C957F2Dull;
    unsigned long long* flags_g = (unsigned long long*)(ws + FLAG_OFF) + slot * 8;
    const bool act3 = idx < 28; const int quad = 4 * xcd + idx / 7, nb = idx % 7, p3 = quad >> 4;
    const __hip_bfloat16* wo3 = (p3 ? woB : woA) + nb * 64 + (((lane & 7) >> 2) ^ ((lane >> 4) & 1)) * 32 + (lane & 3) * 8 + (size_t)(lane >> 3) * DM;
#define WO_INS(ii) glds16(wo3 + (size_t)(ii) * 8 * DM, lds + (ii) * 1024 + ((ii) >= 32 ? 32768 : 0))
#define WO_STAGE1() do { _Pragma("unroll") for (int i_ = 0; i_ < 2; ++i_) { WO_INS(2 * w + i_); WO_INS(32 + 2 * w + i_); } } while (0)
#define WO_STAGE2() do { _Pragma("unroll") for (int i_ = 0; i_ < 2; ++i_) WO_INS(16 + 2 * w + i_); WO_INS(48 + w); } while (0)
    v0 = ldsr16(rd + 2048); v1 = ldsr16(rd + 3072);
    k0 = ldsr16(rd + 4096); k1 = ldsr16(rd + 4096 + 1024);
#pragma unroll
    for (int t = 0; t < 4; ++t) {
        if (t == 1) {
            WAITV(0); BAR();
            if (tid == 0) __hip_atomic_store(flags_g + qblk, token, __ATOMIC_RELAXED, __HIP_MEMORY_SCOPE_AGENT);
        }
        if (t == 2) {
            for (unsigned spins = 0; spins < (1u << 16); ++spins) {
                const unsigned long long f = __hip_atomic_load(flags_g + (lane & 7), __ATOMIC_RELAXED, __HIP_MEMORY_SCOPE_AGENT);
                if (__all(f == token)) break;
                __builtin_amdgcn_s_sleep(2);
            }
            A_STAGE(1); A_STAGE(2); A_STAGE(3);
        }
        if (t < 3) { sAn = MFMA(k0, qA0, z); sAn = MFMA(k1, qA1p, sAn); sBn = MFMA(k0, qB0, z); sBn = MFMA(k1, qB1p, sBn);
            v0n = ldsr16(rd + (t + 1) * 4096 + 2048); v1n = ldsr16(rd + (t + 1) * 4096 + 3072); }
        if (t < 2) { k0 = ldsr16(rd + (t + 2) * 4096); k1 = ldsr16(rd + (t + 2) * 4096 + 1024); }
        EXP16(sA);
        oA = MFMA(v0, pack8(sA, 0), oA); oA = MFMA(v1, pack8(sA, 1), oA);
        EXP16(sB);
        oB = MFMA(v0, pack8(sB, 0), oB); oB = MFMA(v1, pack8(sB, 1), oB);
        if (t < 3) { sA = sAn; sB = sBn; v0 = v0n; v1 = v1n; }
    }
    WAITV(8); BAR();
    k0 = ldsr16(rd + A_CH); k1 = ldsr16(rd + A_CH + 1024);
    sA = MFMA(k0, qA0, z); sA = MFMA(k1, qA1p, sA); sB = MFMA(k0, qB0, z); sB = MFMA(k1, qB1p, sB);
    v0 = ldsr16(rd + A_CH + 2048); v1 = ldsr16(rd + A_CH + 3072);
    k0 = ldsr16(rd + A_CH + 4096); k1 = ldsr16(rd + A_CH + 4096 + 1024);
#pragma unroll
    for (int c = 1; c < 8; ++c) {
        const lds_cptr cb = rd + (c & 3) * A_CH, cn = rd + ((c + 1) & 3) * A_CH;
#pragma unroll
        for (int t = 0; t < 4; ++t) {
            const bool last = (c == 7 && t == 3);
            if (t == 2 && c < 7) {
                if (c < 6) WAITV(4); else WAITV(0);
                BAR();
                if (c + 3 < 8) A_STAGE(c + 3);
                if (act3) { if (c == 5) WO_STAGE1(); if (c == 6) WO_STAGE2(); }
            }
            if (!last) { sAn = MFMA(k0, qA0, z); sAn = MFMA(k1, qA1p, sAn); sBn = MFMA(k0, qB0, z); sBn = MFMA(k1, qB1p, sBn); }
            if (!last) { const lds_cptr vp = (t < 3) ? cb + (t + 1) * 4096 : cn; v0n = ldsr16(vp + 2048); v1n = ldsr16(vp + 3072); }
            if (!(c == 7 && t >= 2)) { const lds_cptr kp = (t < 2) ? cb + (t + 2) * 4096 : cn + (t - 2) * 4096; k0 = ldsr16(kp); k1 = ldsr16(kp + 1024); }
            EXP16(sA);
            oA = MFMA(v0, pack8(sA, 0), oA); oA = MFMA(v1, pack8(sA, 1), oA);
            EXP16(sB);
            oB = MFMA(v0, pack8(sB, 0), oB); oB = MFMA(v1, pack8(sB, 1), oB);
            sA = sAn; sB = sBn; v0 = v0n; v1 = v1n;
        }
    }
    __attribute__((address_space(3))) int* flags = (__attribute__((address_space(3))) int*)(lds + F_FLAGS);
    const bool bad = h && !(oA[12] < 1.0e24f && oB[12] < 1.0e24f && oA[12] > 1.0e-18f && oB[12] > 1.0e-18f);
    const int anybad = __any(bad);
    if (lane == 0) flags[w] = anybad;
    BAR();
    typedef __attribute__((ext_vector_type(4))) float f32x4;
    __attribute__((address_space(3))) f32x4* xw = (__attribute__((address_space(3))) f32x4*)(lds + 32768 + (qg * 2 + kh) * 4096);
    const __attribute__((address_space(3))) f32x4* xr = (const __attribute__((address_space(3))) f32x4*)(lds + 32768 + (qg * 2 + (kh ^ 1)) * 4096);
    __attribute__((address_space(3))) float* mxw = (__attribute__((address_space(3))) float*)(lds + F_MX) + (qg * 2 + kh) * 64;
    const __attribute__((address_space(3))) float* mxr = (const __attribute__((address_space(3))) float*)(lds + F_MX) + (qg * 2 + (kh ^ 1)) * 64;
#define A_PUT() do { const f32x16& og = kh ? oA : oB; _Pragma("unroll") for (int g = 0; g < 4; ++g) xw[g * 64 + lane] = f32x4{og[4 * g], og[4 * g + 1], og[4 * g + 2], og[4 * g + 3]}; \
        mxw[lane] = kh ? mA : mB; } while (0)
    A_PUT();
    const int redo = __any(flags[lane & 7]);
    BAR();
    if (redo) {
        WAITV(0); BAR();
        attn_safe(kvh, qblk, dst, rd, qA0, qA1, qB0, qB1, oA, oB, mA, mB);
        BAR();
        A_PUT();
        if (act3) { WO_STAGE1(); WO_STAGE2(); }
        BAR();
    }
    {
        f32x16 o = kh ? oB : oA; const float m = kh ? mB : mA;
        const float m2 = mxr[lane], mn = fmaxf(m, m2), a1 = __builtin_amdgcn_exp2f(m - mn), a2 = __builtin_amdgcn_exp2f(m2 - mn);
#pragma unroll
        for (int g = 0; g < 4; ++g) { const f32x4 t = xr[g * 64 + lane];
#pragma unroll
            for (int j = 0; j < 4; ++j) o[4 * g + j] = a1 * o[4 * g + j] + a2 * t[j]; }
        const float lo = swap_halves(o[12]);
        const float l = h ? o[12] : lo, inv = 1.0f / l;
#pragma unroll
        for (int r = 0; r < 16; ++r) o[r] *= inv;
        if (h) {
#pragma unroll
            for (int r = 12; r < 16; ++r) o[r] = 0.f;
        }
        char* od = ws + OF_OFF + ((size_t)(((slot >> 4) * 64 + T0 + kh) * 16 + (slot & 15))) * 2048 + lane * 16;
        st16(od, pack8(o, 0)); st16(od + 1024, pack8(o, 1));
    }
    unsigned long long* oflags = (unsigned long long*)(ws + FLAG_OFF + 4096) + ((slot >> 4) * 8 + qblk) * 16;
    WAITV(0); BAR();
    if (tid == 0) __hip_atomic_store(oflags + head, token, __ATOMIC_RELAXED, __HIP_MEMORY_SCOPE_AGENT);
    if (!act3) return;
    {
        const int tl = w & 3, hh = w >> 2, T = (quad & 15) * 4 + tl;
        const int q = (lane & 15) >> 2, cb = ((lane >> 4) & 1) * 32 + (lane & 3) * 8, kb = hh * 224 + 4 * h + q;
        bf16x8 wf0[16], wf1[16];
#pragma unroll
        for (int i = 0; i < 16; ++i) {
            s16x4 f0[2], f1[2];
#pragma unroll
            for (int r = 0; r < 2; ++r) { int k = kb + (i >> 1) * 28 + 16 * (i & 1) + 8 * r; if (i == 15 && r == 1) k = min(k, 447);
                const lds_cptr rowp = lds + k * 128 + ((k >> 8) << 15) + cb; const int sw = ((k >> 1) & 1) * 64;
                f0[r] = ldstr(rowp + sw); f1[r] = ldstr(rowp + (sw ^ 64)); }
            wf0[i] = cat4(f0[0], f0[1]); wf1[i] = cat4(f1[0], f1[1]);
        }
        {
            const unsigned long long* of3 = (const unsigned long long*)(ws + FLAG_OFF + 4096) + (quad >> 1) * 16 + hh * 8;
            for (unsigned spins = 0; spins < (1u << 16); ++spins) {
                const unsigned long long f = __hip_atomic_load(of3 + (lane & 7), __ATOMIC_RELAXED, __HIP_MEMORY_SCOPE_AGENT);
                if (__all(f == token)) break;
                __builtin_amdgcn_s_sleep(2);
            }
        }
        asm volatile("" ::: "memory");
        const auto rs = __builtin_amdgcn_make_buffer_rsrc((void*)(ws + OF_OFF), 0, 4 << 20, 0x00020000);
        const int obase = ((p3 * 64 + T) * 16 + hh * 8) * 2048 + lane * 16;
        bf16x8 of[16];
#pragma unroll
        for (int i = 0; i < 16; ++i) of[i] = __builtin_bit_cast(bf16x8, __builtin_amdgcn_raw_buffer_load_b128(rs, obase + i * 1024, 0, 16));
        f32x16 acc0 = {}, acc1 = {};
#pragma unroll
        for (int i = 0; i < 16; ++i) { acc0 = MFMA(wf0[i], of[i], acc0); acc1 = MFMA(wf1[i], of[i], acc1); }
        const f32x16& give = hh ? acc0 : acc1; f32x16 keep = hh ? acc1 : acc0;
#pragma unroll
        for (int g = 0; g < 4; ++g) xw[g * 64 + lane] = f32x4{give[4 * g], give[4 * g + 1], give[4 * g + 2], give[4 * g + 3]};
        BAR();
        u32x2 pk[4];
#pragma unroll
        for (int g = 0; g < 4; ++g) { const f32x4 t = xr[g * 64 + lane];
            pk[g] = u32x2{pk2(keep[4 * g] + t[0], keep[4 * g + 1] + t[1]), pk2(keep[4 * g + 2] + t[2], keep[4 * g + 3] + t[3])}; }
        __hip_bfloat16* od = out + (size_t)(T * 32 + r32) * 896 + p3 * 448 + nb * 64 + hh * 32 + 8 * h;
#pragma unroll
        for (int g = 0; g < 4; g += 2) {
            const auto rx = __builtin_amdgcn_permlane32_swap(pk[g][0], pk[g + 1][0], false, false);
            const auto ry = __builtin_amdgcn_permlane32_swap(pk[g][1], pk[g + 1][1], false, false);
            *(u32x4*)(od + 8 * g) = u32x4{rx[0], ry[0], rx[1], ry[1]};
        }
    }
}

extern "C" void kernel_launch(void* const* d_in, const int* in_sizes, int n_in, void* d_out, int out_size, void* d_ws, size_t ws_size, hipStream_t stream) {
    const __hip_bfloat16* xA = (const __hip_bfloat16*)d_in[0]; const __hip_bfloat16* wA = (const __hip_bfloat16*)d_in[1]; const __hip_bfloat16* woA = (const __hip_bfloat16*)d_in[2];
    const __hip_bfloat16* xB = (const __hip_bfloat16*)d_in[3]; const __hip_bfloat16* wB = (const __hip_bfloat16*)d_in[4]; const __hip_bfloat16* woB = (const __hip_bfloat16*)d_in[5];
    char* ws = (char*)d_ws;
    fused_kernel<<<256, 512, 0, stream>>>(xA, wA, woA, xB, wB, woB, ws, (__hip_bfloat16*)d_out);
}
```
